```python
import torch

def _softmax(x, axis=-1):
    return torch.softmax(x, dim=axis)
def _gelu(x):
    return torch.nn.functional.gelu(x, approximate="tanh")
def _silu(x):
    return torch.nn.functional.silu(x)
def _sigmoid(x):
    return torch.sigmoid(x)
def _layernorm(x, axis=-1, eps=1e-5):
    m = x.mean(dim=axis, keepdim=True)
    v = x.var(dim=axis, keepdim=True, unbiased=False)
    return (x - m) / torch.sqrt(v + eps)
def _scatter_add(tgt, idx, src, axis=0):
    return tgt.clone().index_add_(axis, idx, src)
def _segment_sum(src, seg, n):
    out = torch.zeros((n,) + src.shape[1:], dtype=src.dtype)
    return out.index_add_(0, seg, src)
def _pad(x, pw):
    flat = tuple(v for pair in reversed(pw) for v in pair)
    return torch.nn.functional.pad(x, flat)

kernel_name = 'parallel_sum__spectral_gfnet__glu_gated_bilinear__prompt'

def setup_inputs():

    _w = (lambda a: (lambda t: t.to(torch.bfloat16) if t.is_floating_point() else t)(torch.as_tensor(a)))
    def _A_setup():
        B, S, D = 1, 2048, 512
        Df = D // 2 + 1
        return {
            "x": _w((lambda *s: torch.randn(*s, dtype=torch.bfloat16) * 0.05)(B, S, D)),
            "w_r": _w((lambda *s: torch.randn(*s, dtype=torch.bfloat16) * 0.4)(S, Df) * 0.02),
            "w_i": _w((lambda *s: torch.randn(*s, dtype=torch.bfloat16) * 0.4)(S, Df) * 0.02),
        }
    _A_in = _A_setup()

    def _B_setup():
        B, S, D, F = 1, 2048, 512, 1792
        return {
            "x": _w((lambda *s: torch.randn(*s, dtype=torch.bfloat16) * 0.05)(B, S, D)),
            "w_gate": _w((lambda *s: torch.randn(*s, dtype=torch.bfloat16) * 0.2)(D, F) * (D ** -0.5)),
            "w_up": _w((lambda *s: torch.randn(*s, dtype=torch.bfloat16) * 0.2)(D, F) * (D ** -0.5)),
            "w_down": _w((lambda *s: torch.randn(*s, dtype=torch.bfloat16) * 0.2)(F, D) * (F ** -0.5)),
        }
    _B_in = _B_setup()

    return {
        "A_x": _A_in["x"],
        "A_w_r": _A_in["w_r"],
        "A_w_i": _A_in["w_i"],
        "B_x": _B_in["x"],
        "B_w_gate": _B_in["w_gate"],
        "B_w_up": _B_in["w_up"],
        "B_w_down": _B_in["w_down"],
    }

def reference(A_x, A_w_r, A_w_i, B_x, B_w_gate, B_w_up, B_w_down):
    def _A_ref(x, w_r, w_i):
        B, S, D = x.shape
        X = (lambda x, axes=(-2,-1): torch.fft.rfft2(x.float(), dim=axes))(x, axes=(-2, -1))
        W = w_r + 1j * w_i
        X = X * W
        y = (lambda x, s, axes=(-2,-1): torch.fft.irfft2(x, s=s, dim=axes).to(torch.bfloat16))(X, (S, D), axes=(-2, -1))
        return y

    def _B_ref(x, w_gate, w_up, w_down):
        g = torch.einsum("bsd,df->bsf", x, w_gate)
        u = torch.einsum("bsd,df->bsf", x, w_up)
        h = g * u
        return torch.einsum("bsf,fd->bsd", h, w_down)

    x = A_x
    xn = _layernorm(x, axis=-1)
    return x + _A_ref(xn, A_w_r, A_w_i) + _B_ref(xn, B_w_gate, B_w_up, B_w_down)
```

```cpp
#include <hip/hip_runtime.h>
#include <hip/hip_bf16.h>
#include <stdint.h>

typedef unsigned short u16;
__device__ __forceinline__ float bf2f(u16 v){ return __uint_as_float(((unsigned)v)<<16); }
__device__ __forceinline__ u16 f2bf(float f){ unsigned u=__float_as_uint(f); u += 0x7FFF + ((u>>16)&1); return (u16)(u>>16); }
__device__ __forceinline__ float rbf(float f){ return bf2f(f2bf(f)); }

constexpr int S=2048, D=512, F=1792, DF=257;

__global__ void k_ln(const u16* x, u16* xn){
  int s=blockIdx.x; int t=threadIdx.x;
  __shared__ float red[512];
  float v=bf2f(x[s*D+t]);
  red[t]=v; __syncthreads();
  for(int o=256;o>0;o>>=1){ if(t<o) red[t]+=red[t+o]; __syncthreads(); }
  float mean=red[0]/D; __syncthreads();
  float d=v-mean; red[t]=d*d; __syncthreads();
  for(int o=256;o>0;o>>=1){ if(t<o) red[t]+=red[t+o]; __syncthreads(); }
  float var=red[0]/D;
  float m=rbf(mean), vb=rbf(var);
  float dd=rbf(v-m);
  float sq=rbf(sqrtf(rbf(vb+1e-5f)));
  xn[s*D+t]=f2bf(dd/sq);
}
__global__ void k_dftD(const u16* xn, float2* U){
  int s=blockIdx.x; int k=threadIdx.x;
  __shared__ float xs[D];
  for(int i=threadIdx.x;i<D;i+=blockDim.x) xs[i]=bf2f(xn[s*D+i]);
  __syncthreads();
  if(k>=DF) return;
  float re=0,im=0;
  for(int d=0;d<D;++d){ int a=(k*d)&511; float sn,cs; sincospif(a/256.0f,&sn,&cs); re+=xs[d]*cs; im-=xs[d]*sn; }
  U[s*DF+k]=make_float2(re,im);
}
__global__ void k_dftS(const float2* in, float2* out, const u16* wr, const u16* wi, int fwd){
  int f=blockIdx.x; int k=threadIdx.x;
  __shared__ float2 tw[S];
  for(int i=threadIdx.x;i<S;i+=blockDim.x){ float sn,cs; sincospif(((f*i)&2047)/1024.0f,&sn,&cs); tw[i]=make_float2(cs, fwd? -sn: sn);}
  __syncthreads();
  if(k>=DF) return;
  float re=0,im=0;
  for(int s=0;s<S;++s){ float2 v=in[s*DF+k]; float2 w=tw[s]; re+=v.x*w.x-v.y*w.y; im+=v.x*w.y+v.y*w.x; }
  if(fwd){ float a=bf2f(wr[f*DF+k]), b=bf2f(wi[f*DF+k]); float r2=re*a-im*b, i2=re*b+im*a; re=r2; im=i2; }
  else { re*=1.0f/S; im*=1.0f/S; }
  out[f*DF+k]=make_float2(re,im);
}
__global__ void k_gu(const u16* xn, const u16* wg, const u16* wu, u16* h){
  int s=blockIdx.x;
  __shared__ float xs[D];
  for(int i=threadIdx.x;i<D;i+=blockDim.x) xs[i]=bf2f(xn[s*D+i]);
  __syncthreads();
  for(int f=threadIdx.x; f<F; f+=blockDim.x){
    float g=0,u=0;
    for(int d=0;d<D;++d){ g+=xs[d]*bf2f(wg[d*F+f]); u+=xs[d]*bf2f(wu[d*F+f]); }
    h[s*F+f]=f2bf(rbf(g)*rbf(u));
  }
}
__global__ void k_out(const u16* x, const float2* Z, const u16* h, const u16* wd, u16* out){
  int s=blockIdx.x; int d=threadIdx.x;
  __shared__ float hs[F]; __shared__ float2 zs[DF];
  for(int i=threadIdx.x;i<F;i+=blockDim.x) hs[i]=bf2f(h[s*F+i]);
  for(int i=threadIdx.x;i<DF;i+=blockDim.x) zs[i]=Z[s*DF+i];
  __syncthreads();
  float b=0;
  for(int f=0;f<F;++f) b+=hs[f]*bf2f(wd[f*D+d]);
  float y=zs[0].x + ((d&1)? -zs[256].x : zs[256].x);
  for(int k=1;k<256;++k){ int a=(k*d)&511; float sn,cs; sincospif(a/256.0f,&sn,&cs); y+=2.0f*(zs[k].x*cs - zs[k].y*sn); }
  y*=1.0f/D;
  float r=rbf(bf2f(x[s*D+d])+rbf(y));
  out[s*D+d]=f2bf(r+rbf(b));
}

extern "C" void kernel_launch(void* const* d_in, const int* in_sizes, int n_in,
                              void* d_out, int out_size, void* d_ws, size_t ws_size,
                              hipStream_t stream) {
  const u16* x=(const u16*)d_in[0]; const u16* wr=(const u16*)d_in[1]; const u16* wi=(const u16*)d_in[2];
  const u16* wg=(const u16*)d_in[4]; const u16* wu=(const u16*)d_in[5]; const u16* wd=(const u16*)d_in[6];
  char* ws=(char*)d_ws;
  u16* xn=(u16*)ws; float2* U=(float2*)(ws+(4<<20)); float2* X=(float2*)(ws+(12<<20)); float2* Z=(float2*)(ws+(20<<20)); u16* h=(u16*)(ws+(28<<20));
  k_ln<<<S,512,0,stream>>>(x,xn);
  k_dftD<<<S,320,0,stream>>>(xn,U);
  k_dftS<<<S,320,0,stream>>>(U,X,wr,wi,1);
  k_dftS<<<S,320,0,stream>>>(X,Z,wr,wi,0);
  k_gu<<<S,256,0,stream>>>(xn,wg,wu,h);
  k_out<<<S,512,0,stream>>>(x,Z,h,wd,(u16*)d_out);
}
```

```cpp
#include <hip/hip_runtime.h>
#include <hip/hip_bf16.h>
#include <stdint.h>

typedef unsigned short u16;
typedef unsigned int u32;
typedef short s16x4 __attribute__((ext_vector_type(4)));
typedef short s16x8 __attribute__((ext_vector_type(8)));
typedef __bf16 bf16x2 __attribute__((ext_vector_type(2)));
typedef __bf16 bf16x8 __attribute__((ext_vector_type(8)));
typedef float f32x16 __attribute__((ext_vector_type(16)));
typedef float f32x4 __attribute__((ext_vector_type(4)));
typedef float cf __attribute__((ext_vector_type(2)));
typedef u32 u32x2 __attribute__((ext_vector_type(2)));
typedef u32 u32x4 __attribute__((ext_vector_type(4)));
#define LAS __attribute__((address_space(3)))
#define GAS __attribute__((address_space(1)))

constexpr int S_ = 2048, D_ = 512, F_ = 1792, DF_ = 257;

#if defined(__HIP_DEVICE_COMPILE__)
constexpr double CT_PI = 3.14159265358979323846264338327950288;
constexpr double ct_sin_small(double x){ double x2=x*x, term=x, sum=x; for(int i=1;i<12;++i){ term*=-x2/((2*i)*(2*i+1)); sum+=term; } return sum; }
constexpr double ct_cos_small(double x){ double x2=x*x, term=1, sum=1; for(int i=1;i<12;++i){ term*=-x2/((2*i-1)*(2*i)); sum+=term; } return sum; }
constexpr double ct_cos512(int j){
  j&=511; if(j>256) j=512-j;
  bool neg=false; if(j>128){ j=256-j; neg=true; }
  double v=(j<=64)? ct_cos_small(2*CT_PI*j/512.0) : ct_sin_small(2*CT_PI*(128-j)/512.0);
  return neg? -v: v;
}
constexpr u16 ct_f2bf(double d){ float f=(float)d; unsigned u=__builtin_bit_cast(unsigned,f); u+=0x7FFFu+((u>>16)&1u); return (u16)(u>>16); }
struct CosTab{ u16 v[512]; };
constexpr CosTab mk_cos(){ CosTab t{}; for(int j=0;j<512;++j){ double c=ct_cos512(j); if(c<1e-12&&c>-1e-12) c=0; t.v[j]=ct_f2bf(c);} return t; }
constexpr CosTab COS_=mk_cos();
struct Chunk{ u16 v[64*512]; };
constexpr Chunk mk_fwd(int r0){ Chunk t{}; for(int d=0;d<64;++d) for(int c=0;c<512;++c){ int dd=r0+d; int a=(c<=256)? c*dd : (c-256)*dd+128; t.v[d*512+c]=COS_.v[a&511]; } return t; }
constexpr Chunk mk_inv(int r0){ Chunk t{}; for(int c=0;c<64;++c) for(int d=0;d<512;++d){ int cc=r0+c; int a=(cc<=256)? cc*d : (cc-256)*d+128; t.v[c*512+d]=COS_.v[a&511]; } return t; }
struct Big{ Chunk c[8]; };
constexpr Chunk cf0=mk_fwd(0),cf1=mk_fwd(64),cf2=mk_fwd(128),cf3=mk_fwd(192),cf4=mk_fwd(256),cf5=mk_fwd(320),cf6=mk_fwd(384),cf7=mk_fwd(448);
constexpr Chunk ci0=mk_inv(0),ci1=mk_inv(64),ci2=mk_inv(128),ci3=mk_inv(192),ci4=mk_inv(256),ci5=mk_inv(320),ci6=mk_inv(384),ci7=mk_inv(448);
static __device__ const __attribute__((aligned(4096))) Big FFWD_={{cf0,cf1,cf2,cf3,cf4,cf5,cf6,cf7}};
static __device__ const __attribute__((aligned(4096))) Big FINV_={{ci0,ci1,ci2,ci3,ci4,ci5,ci6,ci7}};
#define FFWD_PTR ((const char*)&FFWD_)
#define FINV_PTR ((const char*)&FINV_)
#else
#define FFWD_PTR ((const char*)0)
#define FINV_PTR ((const char*)0)
#endif

__device__ __forceinline__ float bf2f(u16 v){ return __uint_as_float(((u32)v)<<16); }
__device__ __forceinline__ float bflo(u32 v){ return __uint_as_float(v<<16); }
__device__ __forceinline__ float bfhi(u32 v){ return __uint_as_float(v&0xffff0000u); }
__device__ __forceinline__ u32 pack2(float lo, float hi){ bf16x2 q; q[0]=(__bf16)lo; q[1]=(__bf16)hi; return __builtin_bit_cast(u32,q); }
__device__ __forceinline__ float rbf(float f){ __bf16 q=(__bf16)f; return (float)q; }

__device__ __forceinline__ void dma16(u32 voff, const void* sbase, u32 lds){
  asm volatile("s_mov_b32 m0, %2\n\ts_nop 0\n\tglobal_load_lds_dwordx4 %0, %1" :: "v"(voff), "s"(sbase), "s"(lds) : "memory");
}
#define WAITV_BAR(n) asm volatile("s_waitcnt vmcnt(" #n ") lgkmcnt(0)\n\ts_barrier" ::: "memory")
#define SCHEDB() __builtin_amdgcn_sched_barrier(0)
#define SGB(m,n) __builtin_amdgcn_sched_group_barrier(m,n,0)
#define ILV() do{ SGB(8,1); SGB(2,2); SGB(8,1); SGB(2,2); SGB(8,1); SGB(2,2); SGB(8,1); SGB(2,2); }while(0)
#define ILV2() do{ SGB(8,1); SGB(2,4); SGB(8,1); SGB(2,4); SGB(8,1); SGB(2,4); SGB(8,1); SGB(2,4); }while(0)

__device__ __forceinline__ bf16x8 lds_row(u32 addr){ return __builtin_bit_cast(bf16x8, *(const LAS u32x4*)(addr)); }
__device__ __forceinline__ bf16x8 lds_tr(u32 a0, u32 a1){
  s16x4 a=__builtin_amdgcn_ds_read_tr16_b64_v4i16((LAS s16x4*)(a0));
  s16x4 b=__builtin_amdgcn_ds_read_tr16_b64_v4i16((LAS s16x4*)(a1));
  return __builtin_bit_cast(bf16x8, __builtin_shufflevector(a,b,0,1,2,3,4,5,6,7));
}
#define MFMA(a,b,c) __builtin_amdgcn_mfma_f32_32x32x16_bf16(a,b,c,0,0,0)
__device__ __forceinline__ float dot2(u32 a, u32 b, float c){ return __builtin_amdgcn_fdot2_f32_bf16(__builtin_bit_cast(bf16x2,a),__builtin_bit_cast(bf16x2,b),c,false); }

constexpr size_t WS_HT = 0;
constexpr size_t WS_ZT = WS_HT + (size_t)F_*S_*2;
constexpr size_t WS_UT = WS_ZT + (size_t)D_*S_*2;
constexpr size_t WS_WP = WS_UT + (size_t)D_*S_*2;
constexpr size_t WS_FL = WS_WP + (size_t)DF_*8192;
constexpr size_t WS_END = WS_FL + 4096;

constexpr u32 A_SLOT0 = 0, B_SLOT0 = 3*16384, STAT_OFF = 9*16384;
constexpr u32 KA_LDS = STAT_OFF + 1024;

struct KAFrag { bf16x8 a0,a1,bg,bu; };

__global__ __launch_bounds__(512,2) void kA(const u16* __restrict__ x, const u16* __restrict__ wr, const u16* __restrict__ wi,
                                            const u16* __restrict__ wg, const u16* __restrict__ wu, char* __restrict__ ws){
  extern __shared__ __attribute__((aligned(16))) char smem[];
  const u32 lds0 = (u32)(size_t)(LAS char*)smem;
  const int tid = threadIdx.x, lane = tid & 63;
  const int wid = __builtin_amdgcn_readfirstlane(tid >> 6);
  const int wm = wid >> 2, wn = wid & 3;
  const int b = blockIdx.x, xcd = b & 7, idx = b >> 3;
  const int mt = 8*(xcd & 1) + (idx & 7), nt = 4*(xcd >> 1) + (idx >> 3);
  const int p0 = mt*128;

  const int f0 = (b >> 5) + 8*((b >> 2) & 7) + 512*(((b & 3) >> 1) | ((b & 1) << 1));
  u32 wre[8], wim[8];
  {
    u32 idx[8];
    #pragma unroll
    for (int j = 0; j < 8; ++j) idx[j] = (u32)((f0 + 64*j)*DF_ + (tid & 255));
    if (tid >= 256) {
      int j = (tid - 256) & 7; int f = f0 + 64*j; int fm = (S_ - f) & (S_-1);
      idx[0] = f*DF_; idx[1] = fm*DF_; idx[2] = f*DF_ + 256; idx[3] = fm*DF_ + 256;
      idx[4] = idx[0]; idx[5] = idx[0]; idx[6] = idx[0]; idx[7] = idx[0];
    }
    if (wid < 5) {
      #pragma unroll
      for (int j = 0; j < 8; ++j) { wre[j] = wr[idx[j]]; wim[j] = wi[idx[j]]; }
    }
  }

  const char* gB; const char* uB; u32 rs;
  if (nt < 14) { gB = (const char*)wg + nt*256; uB = (const char*)wu + nt*256; rs = F_*2; }
  else { gB = FFWD_PTR + (nt-14)*512; uB = gB + 256; rs = D_*2; }
  const char* xB = (const char*)x + (size_t)(p0/8 + 2*wid)*1024;
  const u32 voA0 = (u32)(lane >> 3)*262144u + (u32)(((lane & 7) ^ (lane >> 4))*16);
  const u32 voA1 = (voA0 ^ 64u) + 1024u;
  const u32 voB = (u32)(lane >> 4)*rs + (u32)(((lane & 15) ^ ((lane >> 4) << 2))*16);
  gB += (size_t)(4*wid)*rs; uB += (size_t)(4*wid)*rs;
  auto issueA = [&](int j){ u32 l = lds0 + A_SLOT0 + (j%3)*16384 + wid*2048; const char* s = xB + j*128; dma16(voA0, s, l); dma16(voA1, s, l+1024); };
  auto issueB = [&](int s){ u32 l = lds0 + B_SLOT0 + (s%6)*16384 + wid*1024; size_t o = (size_t)s*32*rs; dma16(voB, gB+o, l); dma16(voB, uB+o, l+8192); };

  issueA(0); issueB(0); issueB(1); issueA(1); issueB(2); issueB(3); issueA(2); issueB(4); issueB(5);

  const int h = lane >> 5, sw = (lane >> 1) & 7;
  u32 aoff[4];
  #pragma unroll
  for (int k = 0; k < 4; ++k) aoff[k] = lds0 + A_SLOT0 + (u32)(wm*64 + (lane & 31))*128 + (u32)(((2*k + h) ^ sw)*16);
  const int q = (lane & 15) >> 2, pp = lane & 3, blk = (lane >> 4) & 1;
  const u32 boff = lds0 + B_SLOT0 + (u32)(8*h + q)*256 + (u32)(((4*wn + 2*blk + (pp >> 1)) ^ (q << 2))*16) + 8*(pp & 1);
  const int srow = tid >> 2;
  const u32 soff0 = lds0 + A_SLOT0 + (u32)srow*128 + (u32)(tid & 3)*32 + (u32)(((srow >> 1) & 1)*16);
  const u32 soff1 = soff0 ^ 16u;

  auto rdfrag = [&](int s, int ks){
    KAFrag f; int j = s >> 1, kk = (s & 1)*2 + ks;
    u32 ab = aoff[kk] + (j%3)*16384;
    f.a0 = lds_row(ab); f.a1 = lds_row(ab + 4096);
    u32 bb = boff + (s%6)*16384 + ks*4096;
    f.bg = lds_tr(bb, bb + 1024); f.bu = lds_tr(bb + 8192, bb + 8192 + 1024);
    return f;
  };

  f32x16 ag0 = {}, ag1 = {}, au0 = {}, au1 = {};
  float csg = 0.f, csu = 0.f, rsum = 0.f, rsq = 0.f;
  const u32 ONES = 0x3f803f80u;
  auto colsum = [&](const KAFrag& f){
    u32x4 g = __builtin_bit_cast(u32x4, f.bg), u = __builtin_bit_cast(u32x4, f.bu);
    #pragma unroll
    for (int i = 0; i < 4; ++i) { csg = dot2(g[i], ONES, csg); csu = dot2(u[i], ONES, csu); }
  };
  auto rowstat = [&](u32x4 v){
    #pragma unroll
    for (int i = 0; i < 4; ++i) { rsum = dot2(v[i], ONES, rsum); rsq = dot2(v[i], v[i], rsq); }
  };
  auto mma = [&](const KAFrag& f){
    ag0 = MFMA(f.a0, f.bg, ag0); au0 = MFMA(f.a0, f.bu, au0); ag1 = MFMA(f.a1, f.bg, ag1); au1 = MFMA(f.a1, f.bu, au1);
  };

  KAFrag F0, F1;
  WAITV_BAR(12);
  F0 = rdfrag(0, 0);
  #pragma unroll
  for (int s = 0; s < 16; ++s) {
    if (s > 0) {
      if (s <= 11) WAITV_BAR(8); else if (s == 12) WAITV_BAR(6); else if (s == 13) WAITV_BAR(2); else if (s == 14) WAITV_BAR(0);
      if (s <= 10) { if ((s & 1) == 0) issueA(s/2 + 2); issueB(s + 5); }
    }
    F1 = rdfrag(s, 1);
    u32x4 sv = *(const LAS u32x4*)(((s & 1) ? soff1 : soff0) + ((s >> 1)%3)*16384);
    SCHEDB();
    mma(F0); colsum(F0);
    asm volatile("" : "+v"(csg), "+v"(csu));
    ILV();
    SCHEDB();
    if (s < 15) F0 = rdfrag(s + 1, 0);
    SCHEDB();
    mma(F1); colsum(F1); rowstat(sv);
    asm volatile("" : "+v"(csg), "+v"(csu), "+v"(rsum), "+v"(rsq));
    ILV2();
    SCHEDB();
  }

  rsum += __shfl_xor(rsum, 1); rsum += __shfl_xor(rsum, 2);
  rsq  += __shfl_xor(rsq, 1);  rsq  += __shfl_xor(rsq, 2);
  if ((tid & 3) == 0) {
    float mean = rsum * (1.0f/D_);
    float var = fmaxf(rsq * (1.0f/D_) - mean*mean, 0.f);
    float mb = rbf(mean);
    float sq = rbf(sqrtf(rbf(rbf(var) + 1e-5f)));
    *(LAS cf*)(lds0 + STAT_OFF + srow*8) = cf{mb, 1.0f/sq};
  }
  csg += __shfl_xor(csg, 32); csu += __shfl_xor(csu, 32);
  asm volatile("s_waitcnt lgkmcnt(0)\n\ts_barrier" ::: "memory");

  const int n = 32*wn + (lane & 31);
  const bool isF = nt >= 14;
  u16* outg = isF ? (u16*)(ws + WS_UT) + (size_t)((nt-14)*256 + n)*S_ : (u16*)(ws + WS_HT) + (size_t)(nt*128 + n)*S_;
  u16* outu = outg + (size_t)128*S_;
  #pragma unroll
  for (int m = 0; m < 2; ++m) {
    const f32x16& G = m ? ag1 : ag0; const f32x16& U = m ? au1 : au0;
    u32 pg[8], pu[8];
    #pragma unroll
    for (int g = 0; g < 4; ++g) {
      int r0 = wm*64 + m*32 + 8*g + 4*h;
      f32x4 s01 = *(const LAS f32x4*)(lds0 + STAT_OFF + r0*8), s23 = *(const LAS f32x4*)(lds0 + STAT_OFF + r0*8 + 16);
      float mean[4] = {s01[0], s01[2], s23[0], s23[2]}, rstd[4] = {s01[1], s01[3], s23[1], s23[3]};
      float gv[4], uv[4];
      #pragma unroll
      for (int e = 0; e < 4; ++e) { gv[e] = rstd[e]*(G[4*g+e] - mean[e]*csg); uv[e] = rstd[e]*(U[4*g+e] - mean[e]*csu); }
      if (isF) { pg[2*g] = pack2(gv[0], gv[1]); pg[2*g+1] = pack2(gv[2], gv[3]); pu[2*g] = pack2(uv[0], uv[1]); pu[2*g+1] = pack2(uv[2], uv[3]); }
      else {
        float hv[4];
        #pragma unroll
        for (int e = 0; e < 4; ++e) hv[e] = rbf(gv[e])*rbf(uv[e]);
        pg[2*g] = pack2(hv[0], hv[1]); pg[2*g+1] = pack2(hv[2], hv[3]);
      }
    }
    #pragma unroll
    for (int gp = 0; gp < 2; ++gp) {
      int po = p0 + wm*64 + m*32 + 16*gp + 8*h;
      { auto r0 = __builtin_amdgcn_permlane32_swap(pg[4*gp], pg[4*gp+2], false, false);
        auto r1 = __builtin_amdgcn_permlane32_swap(pg[4*gp+1], pg[4*gp+3], false, false);
        u32 a0=r0[0], b0=r0[1], a1=r1[0], b1=r1[1];
        *(GAS u32x4*)(outg + po) = u32x4{a0, a1, b0, b1}; }
      if (isF) {
        auto r0 = __builtin_amdgcn_permlane32_swap(pu[4*gp], pu[4*gp+2], false, false);
        auto r1 = __builtin_amdgcn_permlane32_swap(pu[4*gp+1], pu[4*gp+3], false, false);
        u32 a0=r0[0], b0=r0[1], a1=r1[0], b1=r1[1];
        *(GAS u32x4*)(outu + po) = u32x4{a0, a1, b0, b1}; }
    }
  }
  u16* Wp = (u16*)(ws + WS_WP);
  if (wid < 5) {
    #pragma unroll
    for (int j = 0; j < 8; ++j) { asm volatile("" : "+v"(wre[j]), "+v"(wim[j])); }
    if (tid < 256) {
      if (tid > 0) {
        u32 w[8];
        #pragma unroll
        for (int j = 0; j < 8; ++j) w[j] = wre[j] | (wim[j] << 16);
        GAS u32x4* dst = (GAS u32x4*)(Wp + (size_t)tid*4096 + b*16);
        dst[0] = u32x4{w[0], w[1], w[2], w[3]}; dst[1] = u32x4{w[4], w[5], w[6], w[7]};
      }
    } else if (tid < 264) {
      int j = tid - 256;
      float ar = 0.5f*(bflo(wre[0]) + bflo(wre[1])), ai = 0.5f*(bflo(wim[0]) - bflo(wim[1]));
      float br = 0.5f*(bflo(wre[2]) + bflo(wre[3])), bi = 0.5f*(bflo(wim[2]) - bflo(wim[3]));
      *(GAS u32*)(Wp + (size_t)0*4096 + b*16 + 2*j) = pack2(0.5f*(ar + br), 0.5f*(ai + bi));
      *(GAS u32*)(Wp + (size_t)256*4096 + b*16 + 2*j) = pack2(0.5f*(ar - br), 0.5f*(ai - bi));
    }
  }
  if (b == 0 && tid < 256) ((GAS u32*)(ws + WS_FL))[tid] = 0u;
}

__device__ __forceinline__ cf add_mi(cf a, cf b){ cf d; asm("v_pk_add_f32 %0, %1, %2 op_sel:[0,1] op_sel_hi:[1,0] neg_hi:[0,1]" : "=v"(d) : "v"(a), "v"(b)); return d; }
__device__ __forceinline__ cf add_pi(cf a, cf b){ cf d; asm("v_pk_add_f32 %0, %1, %2 op_sel:[0,1] op_sel_hi:[1,0] neg_lo:[0,1]" : "=v"(d) : "v"(a), "v"(b)); return d; }
__device__ __forceinline__ cf cmul(cf a, cf w){ cf t, d;
  asm("v_pk_mul_f32 %0, %1, %2 op_sel:[0,0] op_sel_hi:[0,1]" : "=v"(t) : "v"(a), "v"(w));
  asm("v_pk_fma_f32 %0, %1, %2, %3 op_sel:[1,1,0] op_sel_hi:[1,0,1] neg_lo:[0,1,0]" : "=v"(d) : "v"(a), "v"(w), "v"(t)); return d; }
__device__ __forceinline__ cf cmulc(cf a, cf w){ cf t, d;
  asm("v_pk_mul_f32 %0, %1, %2 op_sel:[0,0] op_sel_hi:[0,1] neg_hi:[0,1]" : "=v"(t) : "v"(a), "v"(w));
  asm("v_pk_fma_f32 %0, %1, %2, %3 op_sel:[1,1,0] op_sel_hi:[1,0,1]" : "=v"(d) : "v"(a), "v"(w), "v"(t)); return d; }
template<int SG> __device__ __forceinline__ cf add_rot(cf a, cf b){ return SG > 0 ? add_mi(a, b) : add_pi(a, b); }
template<int SG> __device__ __forceinline__ cf sub_rot(cf a, cf b){ return SG > 0 ? add_pi(a, b) : add_mi(a, b); }
template<int SG> __device__ __forceinline__ void dft8(cf* x){
  const cf C = {0.70710678118654752f, 0.70710678118654752f};
  cf a0=x[0]+x[4], a1=x[0]-x[4], a2=x[2]+x[6], a3=x[2]-x[6], a4=x[1]+x[5], a5=x[1]-x[5], a6=x[3]+x[7], a7=x[3]-x[7];
  cf e0=a0+a2, e1=a0-a2, o0=a4+a6, o1=a4-a6;
  x[0]=e0+o0; x[4]=e0-o0; x[2]=add_rot<SG>(e1,o1); x[6]=sub_rot<SG>(e1,o1);
  cf r1=add_rot<SG>(a5,a5);
  cf r3=sub_rot<SG>(a7,a7);
  cf f0=add_rot<SG>(a1,a3), f1=sub_rot<SG>(a1,a3);
  cf t0=r1-r3, t1=r1+r3;
  cf c0=C*t0, c1=C*t1;
  x[1]=f0+c0; x[5]=f0-c0; x[3]=add_rot<SG>(f1,c1); x[7]=sub_rot<SG>(f1,c1);
}
__device__ __forceinline__ void twiddles(cf* w, float rev){
  cf w1 = {__builtin_amdgcn_cosf(rev), -__builtin_amdgcn_sinf(rev)};
  w[1]=w1; w[2]=cmul(w1,w1); w[3]=cmul(w[2],w1); w[4]=cmul(w[2],w[2]); w[5]=cmul(w[4],w1); w[6]=cmul(w[3],w[3]); w[7]=cmul(w[4],w[3]);
}
template<int CTRL> __device__ __forceinline__ float dppf(float v){ return __int_as_float(__builtin_amdgcn_update_dpp(0, __float_as_int(v), CTRL, 0xf, 0xf, true)); }

constexpr u32 FFT_ROW = 2304;
constexpr u32 FFT_LDS = 8*FFT_ROW;

template<class SYNC>
__device__ __forceinline__ void fft_column(int k, int u, u32 lds, const char* ws, char* wsw, SYNC sync){
  const int lane = u & 63;
  const u16* UT = (const u16*)(ws + WS_UT);
  const int rre = k, rim = (k == 0) ? 256 : 256 + k;
  u32x4 vre = *(const GAS u32x4*)(UT + (size_t)rre*S_ + u*8);
  u32x4 vim = *(const GAS u32x4*)(UT + (size_t)rim*S_ + u*8);
  const u16* Wp = (const u16*)(ws + WS_WP);
  u32x4 w0 = *(const GAS u32x4*)(Wp + (size_t)k*4096 + u*16), w1 = *(const GAS u32x4*)(Wp + (size_t)k*4096 + u*16 + 8);
  u32x4 q0 = {}, q1 = {};
  if (k == 0) { q0 = *(const GAS u32x4*)(Wp + (size_t)256*4096 + u*16); q1 = *(const GAS u32x4*)(Wp + (size_t)256*4096 + u*16 + 8); }
  cf tw1[8], tw2[8], tw3[8];
  twiddles(tw1, (float)u * (1.0f/2048.0f));
  twiddles(tw2, (float)(u & 31) * (1.0f/256.0f));
  twiddles(tw3, (float)(u & 3) * (1.0f/32.0f));
  cf x[8];
  #pragma unroll
  for (int i = 0; i < 4; ++i) { x[2*i] = cf{bflo(vre[i]), bflo(vim[i])}; x[2*i+1] = cf{bfhi(vre[i]), bfhi(vim[i])}; }
  dft8<1>(x);
  #pragma unroll
  for (int i = 1; i < 8; ++i) x[i] = cmul(x[i], tw1[i]);
  #pragma unroll
  for (int i = 0; i < 8; ++i) *(LAS cf*)(lds + i*FFT_ROW + u*8) = x[i];
  sync();
  const u32 rowb = lds + (u >> 5)*FFT_ROW;
  #pragma unroll
  for (int i = 0; i < 8; ++i) x[i] = *(const LAS cf*)(rowb + ((u & 31) + 32*i)*8);
  dft8<1>(x);
  #pragma unroll
  for (int i = 1; i < 8; ++i) x[i] = cmul(x[i], tw2[i]);
  asm volatile("" ::: "memory");
  #pragma unroll
  for (int i = 0; i < 8; ++i) *(LAS cf*)(rowb + i*288 + (u & 31)*8) = x[i];
  asm volatile("s_waitcnt lgkmcnt(0)" ::: "memory");
  const u32 e2 = rowb + ((u >> 2) & 7)*288 + (u & 3)*8;
  #pragma unroll
  for (int i = 0; i < 8; ++i) x[i] = *(const LAS cf*)(e2 + i*32);
  dft8<1>(x);
  #pragma unroll
  for (int i = 1; i < 8; ++i) x[i] = cmul(x[i], tw3[i]);
  const float sg1 = (lane & 2) ? -1.f : 1.f, sg0 = (lane & 1) ? -1.f : 1.f;
  const bool l3 = (lane & 3) == 3;
  #pragma unroll
  for (int i = 0; i < 8; ++i) {
    cf p = {dppf<0x4E>(x[i].x), dppf<0x4E>(x[i].y)};
    cf v = p + cf{sg1, sg1}*x[i];
    if (l3) v = cf{v.y, -v.x};
    cf p2 = {dppf<0xB1>(v.x), dppf<0xB1>(v.y)};
    x[i] = p2 + cf{sg0, sg0}*v;
  }
  const float sc = (k == 0 ? 1.0f : 2.0f) * (1.0f/(2048.0f*512.0f));
  if (k == 0) {
    const int fb = (u >> 5) + 8*((u >> 2) & 7) + 512*(((u & 3) >> 1) | ((u & 1) << 1));
    sync();
    #pragma unroll
    for (int j = 0; j < 8; ++j) *(LAS cf*)(lds + (fb + 64*j)*8) = x[j];
    sync();
    cf zc[8];
    #pragma unroll
    for (int j = 0; j < 8; ++j) zc[j] = *(const LAS cf*)(lds + ((S_ - (fb + 64*j)) & (S_-1))*8);
    sync();
    #pragma unroll
    for (int j = 0; j < 8; ++j) {
      u32 pw = j < 4 ? w0[j] : w1[j-4], qw = j < 4 ? q0[j] : q1[j-4];
      cf P = cf{bflo(pw), bfhi(pw)}*sc, Q = cf{bflo(qw), bfhi(qw)}*sc;
      cf zcc = cf{zc[j].x, -zc[j].y};
      x[j] = cmul(x[j], P) + cmul(zcc, Q);
    }
  } else {
    #pragma unroll
    for (int j = 0; j < 8; ++j) { u32 pw = j < 4 ? w0[j] : w1[j-4]; x[j] = cmul(x[j], cf{bflo(pw), bfhi(pw)}*sc); }
  }
  #pragma unroll
  for (int i = 0; i < 8; ++i) {
    cf p2 = {dppf<0xB1>(x[i].x), dppf<0xB1>(x[i].y)};
    cf v = p2 + cf{sg0, sg0}*x[i];
    if (l3) v = cf{-v.y, v.x};
    cf p = {dppf<0x4E>(v.x), dppf<0x4E>(v.y)};
    x[i] = p + cf{sg1, sg1}*v;
  }
  #pragma unroll
  for (int i = 1; i < 8; ++i) x[i] = cmulc(x[i], tw3[i]);
  dft8<-1>(x);
  asm volatile("" ::: "memory");
  #pragma unroll
  for (int i = 0; i < 8; ++i) *(LAS cf*)(e2 + i*32) = x[i];
  asm volatile("s_waitcnt lgkmcnt(0)" ::: "memory");
  #pragma unroll
  for (int i = 0; i < 8; ++i) x[i] = *(const LAS cf*)(rowb + i*288 + (u & 31)*8);
  #pragma unroll
  for (int i = 1; i < 8; ++i) x[i] = cmulc(x[i], tw2[i]);
  dft8<-1>(x);
  asm volatile("" ::: "memory");
  #pragma unroll
  for (int i = 0; i < 8; ++i) *(LAS cf*)(rowb + ((u & 31) + 32*i)*8) = x[i];
  sync();
  #pragma unroll
  for (int i = 0; i < 8; ++i) x[i] = *(const LAS cf*)(lds + i*FFT_ROW + u*8);
  #pragma unroll
  for (int i = 1; i < 8; ++i) x[i] = cmulc(x[i], tw1[i]);
  dft8<-1>(x);
  u32x4 ore, oim;
  #pragma unroll
  for (int i = 0; i < 4; ++i) { ore[i] = pack2(x[2*i].x, x[2*i+1].x); oim[i] = pack2(x[2*i].y, x[2*i+1].y); }
  u16* ZT = (u16*)(wsw + WS_ZT);
  *(GAS u32x4*)(ZT + (size_t)rre*S_ + u*8) = ore;
  *(GAS u32x4*)(ZT + (size_t)rim*S_ + u*8) = oim;
}

__global__ __launch_bounds__(256) void kF(char* ws){
  extern __shared__ __attribute__((aligned(16))) char smem[];
  const u32 lds0 = (u32)(size_t)(LAS char*)smem;
  fft_column(blockIdx.x, threadIdx.x, lds0, ws, ws, [](){ asm volatile("s_waitcnt lgkmcnt(0)\n\ts_barrier" ::: "memory"); });
}

struct KGFrag { bf16x8 d0,d1,p0,p1; };
#define WAITV(n) asm volatile("s_waitcnt vmcnt(" #n ")" ::: "memory")
#define WAITL(n) asm volatile("s_waitcnt lgkmcnt(" #n ")" ::: "memory")
template<int N> __device__ __forceinline__ void waitv(){
  if constexpr (N==0) WAITV(0); else if constexpr (N==4) WAITV(4); else if constexpr (N==8) WAITV(8); else if constexpr (N==12) WAITV(12);
  else if constexpr (N==16) WAITV(16); else if constexpr (N==20) WAITV(20); else WAITV(24);
}

template<int I> struct StepLoop {
  template<class Fn> static __device__ __forceinline__ void run(Fn& f){ f.template step<I>(); StepLoop<I+1>::run(f); }
};
template<> struct StepLoop<36> { template<class Fn> static __device__ __forceinline__ void run(Fn&){} };

struct KG {
  const char* aT; const char* wd; u32 voP, voD, ring, rP0, rP1;
  f32x16 acc00 = {}, acc01 = {}, acc10 = {}, acc11 = {};
  KGFrag F[2];
  __device__ __forceinline__ void issue(int i){
    u32 l = ring + (i & 7)*4096;
    const char* sp = aT + (size_t)i*64*4096;
    const char* sd = (i < 28) ? wd + (size_t)i*64*1024 : wd_inv + (size_t)(i-28)*64*1024;
    dma16(voP, sp, l); dma16(voP, sp + 8*4096, l + 1024); dma16(voD, sd, l + 2048); dma16(voD, sd + 8*1024, l + 3072);
  }
  const char* wd_inv;
  __device__ __forceinline__ KGFrag rd(int i){
    KGFrag f; u32 o = (i & 7)*4096;
    f.p0 = lds_tr(rP0 + o, rP0 + o + 512); f.p1 = lds_tr(rP1 + o, rP1 + o + 512);
    f.d0 = lds_tr(rP0 + o + 2048, rP0 + o + 2048 + 512); f.d1 = lds_tr(rP1 + o + 2048, rP1 + o + 2048 + 512);
    return f;
  }
  template<int I> __device__ __forceinline__ void step(){
    if constexpr (I + 1 < 36) {
      constexpr int rem = (35 - (I + 1) < 6) ? 35 - (I + 1) : 6;
      waitv<4*rem>();
      F[(I+1)&1] = rd(I + 1);
      WAITL(8);
    } else WAITL(0);
    if constexpr (I + 8 < 36) issue(I + 8);
    SCHEDB();
    const KGFrag& f = F[I&1];
    acc00 = MFMA(f.d0, f.p0, acc00); acc01 = MFMA(f.d0, f.p1, acc01); acc10 = MFMA(f.d1, f.p0, acc10); acc11 = MFMA(f.d1, f.p1, acc11);
    SCHEDB();
  }
};

__device__ __forceinline__ void gemm2_tile(int b, int w, int lane, u32 lds0, const u16* x, const u16* wd, char* ws, u16* out){
  const int xcd = b & 7, idx = b >> 3;
  const int pt = xcd*4 + (idx & 3), dt = idx >> 2;
  KG g;
  g.aT = ws + WS_HT + (size_t)(16*w)*4096 + pt*128;
  g.wd = (const char*)wd + (size_t)(16*w)*1024 + dt*128;
  g.wd_inv = FINV_PTR + (size_t)(16*w)*1024 + dt*128;
  const u32 ch = (u32)(((lane & 7) ^ (((lane >> 4) & 1) << 2))*16);
  g.voP = (u32)(lane >> 3)*4096u + ch; g.voD = (u32)(lane >> 3)*1024u + ch;
  g.ring = lds0 + w*32768;
  const int h = lane >> 5, q = (lane & 15) >> 2, pp = lane & 3, blk = (lane >> 4) & 1;
  g.rP0 = g.ring + (u32)(8*h + q)*128 + (u32)(((2*blk + (pp >> 1)) ^ ((q & 2) << 1))*16) + 8*(pp & 1);
  g.rP1 = g.rP0 ^ 64u;
  #pragma unroll
  for (int i = 0; i < 8; ++i) g.issue(i);
  waitv<24>(); WAITV(28);
  g.F[0] = g.rd(0);
  StepLoop<0>::run(g);
  {
    auto put = [&](const f32x16& a, int slot){
      #pragma unroll
      for (int r = 0; r < 4; ++r) *(LAS f32x4*)(g.ring + slot*4096 + r*1024 + lane*16) = f32x4{a[4*r], a[4*r+1], a[4*r+2], a[4*r+3]};
    };
    if (w != 0) put(g.acc00, 0); if (w != 1) put(g.acc01, 1); if (w != 2) put(g.acc10, 2); if (w != 3) put(g.acc11, 3);
  }
  asm volatile("s_waitcnt lgkmcnt(0)\n\ts_barrier" ::: "memory");
  f32x16 a = w == 0 ? g.acc00 : w == 1 ? g.acc01 : w == 2 ? g.acc10 : g.acc11;
  #pragma unroll
  for (int o = 1; o < 4; ++o) {
    int ow = (w + o) & 3;
    #pragma unroll
    for (int r = 0; r < 4; ++r) { f32x4 v = *(const LAS f32x4*)(lds0 + ow*32768 + w*4096 + r*1024 + lane*16);
      a[4*r] += v[0]; a[4*r+1] += v[1]; a[4*r+2] += v[2]; a[4*r+3] += v[3]; }
  }
  const int p = pt*64 + (w & 1)*32 + (lane & 31);
  const int s = (p >> 3) + 256*(p & 7);
  const int d0 = dt*64 + (w >> 1)*32;
  #pragma unroll
  for (int gp = 0; gp < 2; ++gp) {
    float v[8];
    #pragma unroll
    for (int e = 0; e < 4; ++e) {
      auto r = __builtin_amdgcn_permlane32_swap(__float_as_uint(a[8*gp + e]), __float_as_uint(a[8*gp + 4 + e]), false, false);
      u32 lo = r[0], hi = r[1]; v[e] = __uint_as_float(lo); v[4+e] = __uint_as_float(hi);
    }
    const size_t off = (size_t)s*D_ + d0 + 16*gp + 8*h;
    u32x4 xv = *(const GAS u32x4*)(x + off);
    u32x4 o;
    #pragma unroll
    for (int e = 0; e < 4; ++e) o[e] = pack2(bflo(xv[e]) + v[2*e], bfhi(xv[e]) + v[2*e+1]);
    *(GAS u32x4*)(out + off) = o;
  }
}

__global__ __launch_bounds__(256,1) void kG(const u16* __restrict__ x, const u16* __restrict__ wd, char* __restrict__ ws, u16* __restrict__ out){
  extern __shared__ __attribute__((aligned(16))) char smem[];
  const u32 lds0 = (u32)(size_t)(LAS char*)smem;
  const int w = __builtin_amdgcn_readfirstlane(threadIdx.x >> 6);
  gemm2_tile(blockIdx.x, w, threadIdx.x & 63, lds0, x, wd, ws, out);
}

extern "C" void kernel_launch(void* const* d_in, const int* in_sizes, int n_in,
                              void* d_out, int out_size, void* d_ws, size_t ws_size,
                              hipStream_t stream) {
  const u16* x=(const u16*)d_in[0]; const u16* wr=(const u16*)d_in[1]; const u16* wi=(const u16*)d_in[2];
  const u16* wg=(const u16*)d_in[4]; const u16* wu=(const u16*)d_in[5]; const u16* wd=(const u16*)d_in[6];
  char* ws=(char*)d_ws;
  (void)hipFuncSetAttribute((const void*)kA, hipFuncAttributeMaxDynamicSharedMemorySize, KA_LDS);
  (void)hipFuncSetAttribute((const void*)kG, hipFuncAttributeMaxDynamicSharedMemorySize, 131072);
  kA<<<256,512,KA_LDS,stream>>>(x,wr,wi,wg,wu,ws);
  kF<<<256,256,FFT_LDS,stream>>>(ws);
  kG<<<256,256,131072,stream>>>(x,wd,ws,(u16*)d_out);
}
```

```cpp
#include <hip/hip_runtime.h>
#include <hip/hip_bf16.h>
#include <stdint.h>

typedef unsigned short u16;
typedef unsigned int u32;
typedef unsigned long long u64;
typedef short s16x4 __attribute__((ext_vector_type(4)));
typedef __bf16 bf16x2 __attribute__((ext_vector_type(2)));
typedef __bf16 bf16x8 __attribute__((ext_vector_type(8)));
typedef float f32x16 __attribute__((ext_vector_type(16)));
typedef float f32x4 __attribute__((ext_vector_type(4)));
typedef float cf __attribute__((ext_vector_type(2)));
typedef u32 u32x2 __attribute__((ext_vector_type(2)));
typedef u32 u32x4 __attribute__((ext_vector_type(4)));
#define LAS __attribute__((address_space(3)))
#define GAS __attribute__((address_space(1)))
#pragma clang diagnostic ignored "-Wint-to-pointer-cast"

#define CDPOS 1
constexpr int S_ = 2048, D_ = 512, F_ = 1792, DF_ = 257, KT_ = 2304;

#if defined(__HIP_DEVICE_COMPILE__)
constexpr double CT_PI = 3.14159265358979323846264338327950288;
constexpr double ct_sin_small(double x){ double x2=x*x, term=x, sum=x; for(int i=1;i<12;++i){ term*=-x2/((2*i)*(2*i+1)); sum+=term; } return sum; }
constexpr double ct_cos_small(double x){ double x2=x*x, term=1, sum=1; for(int i=1;i<12;++i){ term*=-x2/((2*i-1)*(2*i)); sum+=term; } return sum; }
constexpr double ct_cos64(int j){
  j&=63; if(j>32) j=64-j;
  bool neg=false; if(j>16){ j=32-j; neg=true; }
  double v=(j<=8)? ct_cos_small(2*CT_PI*j/64.0) : ct_sin_small(2*CT_PI*(16-j)/64.0);
  if (j==16) v=0;
  return neg? -v: v;
}
constexpr u16 ct_f2bf(double d){ float f=(float)d; unsigned u=__builtin_bit_cast(unsigned,f); u+=0x7FFFu+((u>>16)&1u); return (u16)(u>>16); }
struct DftTab{ u16 v[24*64*8]; };
constexpr DftTab mk_dft(){
  DftTab t{};
  for(int f=0;f<24;++f) for(int l=0;l<64;++l) for(int j=0;j<8;++j){
    const int g=l>>4, r=l&15; int m=0; const bool sn=(f&1)!=0;
    if(f<4){ const int c=f>>1; m=2*((g+4*j)*(16*c+r)); }
    else if(f<8){ const int c=(f-4)>>1; const int n1=(r>>2)+4*(4*c+(r&3)), k1=(j<4? 4*g+j : 12+4*g+j); m=2*(n1*k1); }
    else { const int w=(f-8)>>2, ks=((f-8)>>1)&1; m=(16*w+r)*(32*ks+8*g+j); }
    t.v[(f*64+l)*8+j]=ct_f2bf(sn? ct_cos64(m-16) : ct_cos64(m));
  }
  return t;
}
static __device__ const __attribute__((aligned(1024))) DftTab DFT_=mk_dft();
#define DFT_PTR ((const char*)&DFT_)
#else
#define DFT_PTR ((const char*)0)
#endif

constexpr size_t WS_AB = 0;
constexpr size_t WS_UW = WS_AB + (size_t)32*KT_*128;
constexpr size_t WS_WQ = WS_UW + (size_t)256*16384;
constexpr size_t WS_STM = WS_WQ + 8192;
constexpr size_t WS_STR = WS_STM + 8192;
constexpr size_t WS_CS = WS_STR + 8192;
constexpr size_t WS_FL = WS_CS + 57344;
constexpr u32 FL_E = 0, FL_H = 128, FL_Z = 4096, FL_U = FL_Z + 64*128, FL_S = FL_U + 64*128, FL_ST = FL_S + 256*128;
constexpr size_t WS_END = WS_FL + FL_ST + 256*128;

constexpr u32 L_A = 0, L_B = 3*16384, NBS = 5;
constexpr u32 L_SCR = 131072;
constexpr u32 FFT_ROW = 2304;
constexpr u32 L_X = L_SCR;
constexpr u32 L_W = L_SCR + 18432;
constexpr u32 L_STM = L_W, L_STR = L_W + 512, L_CS = L_W + 1024, L_FS = L_W + 5120;
constexpr u32 L_SP = L_W + 8448;
constexpr u32 L_ST8 = L_SP + 256;
constexpr u32 L_CNT = L_ST8 + 128;
constexpr u32 L_CP = L_CNT + 64;
constexpr u32 LDS_TOTAL = 163840;
constexpr u32 L_BASE = L_CP + 4096;
#define CODE_BYTES 34816
constexpr u32 L_DUM = L_BASE + 256, L_DUM2 = L_DUM + 256;
static_assert(L_DUM2 + 512 <= LDS_TOTAL, "LDS");

__device__ __forceinline__ float bflo(u32 v){ return __uint_as_float(v<<16); }
__device__ __forceinline__ float bfhi(u32 v){ return __uint_as_float(v&0xffff0000u); }
__device__ __forceinline__ u32 pack2(float lo, float hi){ bf16x2 q; q[0]=(__bf16)lo; q[1]=(__bf16)hi; return __builtin_bit_cast(u32,q); }
__device__ __forceinline__ float rbf(float f){ __bf16 q=(__bf16)f; return (float)q; }
__device__ __forceinline__ void dma16(u32 voff, const void* sbase, u32 lds){
  asm volatile("s_mov_b32 m0, %2\n\ts_nop 0\n\tglobal_load_lds_dwordx4 %0, %1" :: "v"(voff), "s"(sbase), "s"(lds) : "memory");
}
__device__ __forceinline__ void dma16_sc1(u32 voff, const void* sbase, u32 lds){
  asm volatile("s_mov_b32 m0, %2\n\ts_nop 0\n\tglobal_load_lds_dwordx4 %0, %1 sc1" :: "v"(voff), "s"(sbase), "s"(lds) : "memory");
}
__device__ __forceinline__ void st16_sc1(void* p, u32x4 v){
  asm volatile("global_store_dwordx4 %0, %1, off sc1\n\ts_nop 1" :: "v"(p), "v"(v) : "memory");
}
__device__ __forceinline__ void st4_sc1(void* p, u32 v){ __hip_atomic_store((GAS u32*)p, v, __ATOMIC_RELAXED, __HIP_MEMORY_SCOPE_AGENT); }
__device__ __forceinline__ u32 ld4_sc1(const void* p){ return __hip_atomic_load((const GAS u32*)p, __ATOMIC_RELAXED, __HIP_MEMORY_SCOPE_AGENT); }
#define SCHEDB() __builtin_amdgcn_sched_barrier(0)
template<int N> __device__ __forceinline__ void waitv(){ asm volatile("s_waitcnt vmcnt(%0)" :: "n"(N) : "memory"); }
template<int N> __device__ __forceinline__ void waitl(){ asm volatile("s_waitcnt lgkmcnt(%0)" :: "n"(N) : "memory"); }
__device__ __forceinline__ void bar(){ asm volatile("s_barrier" ::: "memory"); }
__device__ __forceinline__ void lbar(){ asm volatile("s_waitcnt lgkmcnt(0)\n\ts_barrier" ::: "memory"); }
__device__ __forceinline__ bf16x8 lds_row(u32 addr){ return __builtin_bit_cast(bf16x8, *(const LAS u32x4*)(addr)); }
__device__ __forceinline__ bf16x8 lds_tr(u32 a0, u32 a1){
  s16x4 a=__builtin_amdgcn_ds_read_tr16_b64_v4i16((LAS s16x4*)(a0));
  s16x4 b=__builtin_amdgcn_ds_read_tr16_b64_v4i16((LAS s16x4*)(a1));
  return __builtin_bit_cast(bf16x8, __builtin_shufflevector(a,b,0,1,2,3,4,5,6,7));
}
#define MFMA(a,b,c) __builtin_amdgcn_mfma_f32_32x32x16_bf16(a,b,c,0,0,0)
__device__ __forceinline__ void lds_arrive(u32 addr_v){
  u32 one = 1; u64 sv;
  asm volatile("s_mov_b64 %0, exec\n\ts_mov_b64 exec, 1\n\tds_add_u32 %1, %2\n\ts_mov_b64 exec, %0" : "=&s"(sv) : "v"(addr_v), "v"(one) : "memory");
}
constexpr u32 SPIN_CAP = 1u << 18;
#define TMO(code)
template<int I> struct IC { static constexpr int value = I; };
template<int N, int I = 0, class F> __device__ __forceinline__ void static_for(F& f){ if constexpr (I < N) { f(IC<I>{}); static_for<N, I + 1>(f); } }

#define STAMP(grp, i)
#define PST(i)
#define STAMP_FLUSH()

#define NOPA ""
#define NOPB ""
__device__ __forceinline__ void bfly_rot(cf a, cf b, cf& p, cf& m){
  asm(NOPA "v_pk_add_f32 %0, %2, %3 op_sel:[0,1] op_sel_hi:[1,0] neg_hi:[0,1]\n\t"
      "v_pk_add_f32 %1, %2, %3 op_sel:[0,1] op_sel_hi:[1,0] neg_lo:[0,1]" : "=&v"(p), "=&v"(m) : "v"(a), "v"(b)); }
__device__ __forceinline__ cf add_mi(cf a, cf b){ cf d; asm(NOPA "v_pk_add_f32 %0, %1, %2 op_sel:[0,1] op_sel_hi:[1,0] neg_hi:[0,1]" : "=&v"(d) : "v"(a), "v"(b)); return d; }
__device__ __forceinline__ cf add_pi(cf a, cf b){ cf d; asm(NOPA "v_pk_add_f32 %0, %1, %2 op_sel:[0,1] op_sel_hi:[1,0] neg_lo:[0,1]" : "=&v"(d) : "v"(a), "v"(b)); return d; }
__device__ __forceinline__ cf cmul(cf a, cf w){ cf t, d;
  asm(NOPA "v_pk_mul_f32 %0, %2, %3 op_sel:[0,0] op_sel_hi:[0,1]\n\t" NOPB
      "v_pk_fma_f32 %1, %2, %3, %0 op_sel:[1,1,0] op_sel_hi:[1,0,1] neg_lo:[0,1,0]" : "=&v"(t), "=&v"(d) : "v"(a), "v"(w)); return d; }
__device__ __forceinline__ cf cmulc(cf a, cf w){ cf t, d;
  asm(NOPA "v_pk_mul_f32 %0, %2, %3 op_sel:[0,0] op_sel_hi:[0,1] neg_hi:[0,1]\n\t" NOPB
      "v_pk_fma_f32 %1, %2, %3, %0 op_sel:[1,1,0] op_sel_hi:[1,0,1]" : "=&v"(t), "=&v"(d) : "v"(a), "v"(w)); return d; }
template<int SG> __device__ __forceinline__ void bfly4(cf a, cf b, cf& p, cf& m){ if (SG > 0) bfly_rot(a, b, p, m); else bfly_rot(a, b, m, p); }
template<int SG> __device__ __forceinline__ cf add_rot(cf a, cf b){ return SG > 0 ? add_mi(a, b) : add_pi(a, b); }
template<int SG> __device__ __forceinline__ cf sub_rot(cf a, cf b){ return SG > 0 ? add_pi(a, b) : add_mi(a, b); }
template<int SG> __device__ __forceinline__ void dft8(cf* x){
  const cf C = {0.70710678118654752f, 0.70710678118654752f};
  cf a0=x[0]+x[4], a1=x[0]-x[4], a2=x[2]+x[6], a3=x[2]-x[6], a4=x[1]+x[5], a5=x[1]-x[5], a6=x[3]+x[7], a7=x[3]-x[7];
  cf e0=a0+a2, e1=a0-a2, o0=a4+a6, o1=a4-a6;
  x[0]=e0+o0; x[4]=e0-o0; bfly4<SG>(e1, o1, x[2], x[6]);
  cf r1=add_rot<SG>(a5,a5), r3=sub_rot<SG>(a7,a7);
  cf f0, f1; bfly4<SG>(a1, a3, f0, f1);
  cf t0=r1-r3, t1=r1+r3;
  cf c0=C*t0, c1=C*t1;
  x[1]=f0+c0; x[5]=f0-c0; bfly4<SG>(f1, c1, x[3], x[7]);
}
__device__ __forceinline__ void twiddles(cf* w, float rev){
  cf w1 = {__builtin_amdgcn_cosf(rev), -__builtin_amdgcn_sinf(rev)};
  asm volatile("s_nop 1" : "+v"(w1));
  w[1]=w1; w[2]=cmul(w1,w1); w[3]=cmul(w[2],w1); w[4]=cmul(w[2],w[2]); w[5]=cmul(w[4],w1); w[6]=cmul(w[3],w[3]); w[7]=cmul(w[4],w[3]);
}
template<int CTRL> __device__ __forceinline__ float dppf(float v){ return __int_as_float(__builtin_amdgcn_update_dpp(0, __float_as_int(v), CTRL, 0xf, 0xf, true)); }
__device__ __forceinline__ float wave_sum(float v){
  v += dppf<0x111>(v); v += dppf<0x112>(v); v += dppf<0x114>(v); v += dppf<0x118>(v);
  v += __int_as_float(__builtin_amdgcn_update_dpp(0, __float_as_int(v), 0x142, 0xa, 0xf, false));
  v += __int_as_float(__builtin_amdgcn_update_dpp(0, __float_as_int(v), 0x143, 0xc, 0xf, false));
  return __int_as_float(__builtin_amdgcn_readlane(__float_as_int(v), 63));
}

struct RowStat { float ma, ra, mb, rb; };
__device__ __forceinline__ void r1_mean(u32x4 va4, u32x4 vb4, float& ma, float& mb){
  float sa = ((bflo(va4[0]) + bfhi(va4[0])) + (bflo(va4[1]) + bfhi(va4[1]))) + ((bflo(va4[2]) + bfhi(va4[2])) + (bflo(va4[3]) + bfhi(va4[3])));
  float sb = ((bflo(vb4[0]) + bfhi(vb4[0])) + (bflo(vb4[1]) + bfhi(vb4[1]))) + ((bflo(vb4[2]) + bfhi(vb4[2])) + (bflo(vb4[3]) + bfhi(vb4[3])));
  ma = wave_sum(sa)*(1.0f/D_); mb = wave_sum(sb)*(1.0f/D_);
}
__device__ __forceinline__ RowStat r1_var(u32x4 va4, u32x4 vb4, float ma, float mb){
  float va = 0.f, vb = 0.f;
  #pragma unroll
  for (int i = 0; i < 4; ++i) { float d0 = bflo(va4[i]) - ma, d1 = bfhi(va4[i]) - ma, e0 = bflo(vb4[i]) - mb, e1 = bfhi(vb4[i]) - mb; va += d0*d0 + d1*d1; vb += e0*e0 + e1*e1; }
  va = wave_sum(va)*(1.0f/D_); vb = wave_sum(vb)*(1.0f/D_);
  RowStat s;
  s.ma = rbf(ma); s.mb = rbf(mb);
  s.ra = __builtin_amdgcn_rcpf(rbf(__builtin_amdgcn_sqrtf(rbf(rbf(va) + 1e-5f)))); s.rb = __builtin_amdgcn_rcpf(rbf(__builtin_amdgcn_sqrtf(rbf(rbf(vb) + 1e-5f))));
  return s;
}
struct Side {
  u32x4 g0, g1; u32 sp16; RowStat st; int lane, wid, tid, b; u32 lds0; char* ws;
  cf x[8], tw[8], tw2[8]; u32 ure[4], uim[4], pv;
  __device__ __forceinline__ void pre(){ twiddles(tw, (float)lane*(1.0f/512.0f)); twiddles(tw2, (float)(lane & 7)*(1.0f/64.0f)); }
  template<int K> __device__ __forceinline__ void run(){
    const u32 xb0 = lds0 + L_X + wid*4608;
    const int q = lane >> 3, t = lane & 7, k0 = q + 8*t;
    if (wid < 4) {
      if constexpr (K == 0) {
        #pragma unroll
        for (int i = 0; i < 4; ++i) {
          *(LAS cf*)(xb0 + (9*lane + 2*i)*8)     = cf{bflo(g0[i]), bflo(g1[i])};
          *(LAS cf*)(xb0 + (9*lane + 2*i + 1)*8) = cf{bfhi(g0[i]), bfhi(g1[i])};
        }
      } else if constexpr (K == 1) {
        #pragma unroll
        for (int i = 0; i < 8; ++i) x[i] = *(const LAS cf*)(xb0 + (lane + (lane >> 3) + 72*i)*8);
        dft8<1>(x);
        #pragma unroll
        for (int i = 1; i < 8; ++i) x[i] = cmul(x[i], tw[i]);
        asm volatile("" ::: "memory");
        #pragma unroll
        for (int i = 0; i < 8; ++i) *(LAS cf*)(xb0 + (i*72 + lane)*8) = x[i];
      } else if constexpr (K == 2) {
        #pragma unroll
        for (int i = 0; i < 8; ++i) x[i] = *(const LAS cf*)(xb0 + (q*72 + t + 8*i)*8);
        dft8<1>(x);
        #pragma unroll
        for (int i = 1; i < 8; ++i) x[i] = cmul(x[i], tw2[i]);
        asm volatile("" ::: "memory");
        #pragma unroll
        for (int i = 0; i < 8; ++i) *(LAS cf*)(xb0 + (q*72 + i*9 + t)*8) = x[i];
      } else if constexpr (K == 3) {
        #pragma unroll
        for (int i = 0; i < 8; ++i) x[i] = *(const LAS cf*)(xb0 + (q*72 + t*9 + i)*8);
        dft8<1>(x);
        asm volatile("" ::: "memory");
        #pragma unroll
        for (int i = 0; i < 8; ++i) { int k = k0 + 64*i; *(LAS cf*)(xb0 + (k + (k >> 3))*8) = x[i]; }
      } else if constexpr (K == 4) {
        #pragma unroll
        for (int i = 0; i < 4; ++i) {
          const int k = k0 + 64*i, km = (512 - k) & 511;
          cf zm = *(const LAS cf*)(xb0 + (km + (km >> 3))*8);
          cf z = x[i];
          const float ha = 0.5f*st.ra, hb = 0.5f*st.rb;
          float ar = z.x + zm.x, br = z.y + zm.y;
          if (i == 0 && lane == 0) { ar -= 1024.0f*st.ma; br -= 1024.0f*st.mb; }
          ure[i] = pack2(ha*ar, hb*br);
          uim[i] = pack2(ha*(z.y - zm.y), hb*(zm.x - z.x));
          if (i == 0 && lane == 0) uim[i] = pack2(st.ra*x[4].x, st.rb*x[4].y);
        }
        asm volatile("" : "+v"(ure[0]), "+v"(ure[1]), "+v"(ure[2]), "+v"(ure[3]), "+v"(uim[0]), "+v"(uim[1]), "+v"(uim[2]), "+v"(uim[3]));
      } else if constexpr (K == 5) {
        #pragma unroll
        for (int i = 0; i < 4; ++i) {
          const int k = k0 + 64*i; const u32 rb = lds0 + L_X + k*64 + wid*4; const u32 sw = (k >> 3) & 3;
          *(LAS u32*)(rb + (0u ^ sw)*16) = ure[i];
          *(LAS u32*)(rb + (1u ^ sw)*16) = uim[i];
        }
        waitl<0>();
      }
    } else {
      if constexpr (K == 7) {
        const u32 wt = lds0 + L_W + (u32)(4*(wid - 4) + (lane >> 5))*528 + (lane & 31)*16;
        *(LAS u32x4*)(wt) = g0; *(LAS u32x4*)(wt + 2*528) = g1;
        if (wid == 4) {
          *(LAS u32*)(lds0 + L_SP + lane*4) = sp16;
          u32x4 s0 = *(const LAS u32x4*)(lds0 + L_SP + (lane & 7)*32), s1 = *(const LAS u32x4*)(lds0 + L_SP + (lane & 7)*32 + 16);
          float ar = 0.5f*(bflo(s0[0]) + bflo(s0[2])), ai = 0.5f*(bflo(s0[1]) - bflo(s0[3]));
          float br = 0.5f*(bflo(s1[0]) + bflo(s1[2])), bi = 0.5f*(bflo(s1[1]) - bflo(s1[3]));
          pv = pack2(0.5f*(ar + br), 0.5f*(ai + bi));
          if (lane < 8) st4_sc1(ws + WS_WQ + (size_t)b*32 + lane*4, pack2(0.5f*(ar - br), 0.5f*(ai - bi)));
        }
        waitl<0>();
      } else if constexpr (K == 5) {
        const int kk = tid - 256;
        const u32 ta = lds0 + L_W + (u32)((lane & 15) >> 2)*528 + (u32)((kk & ~15) + (lane & 3)*4)*2;
        s16x4 t0 = __builtin_amdgcn_ds_read_tr16_b64_v4i16((LAS s16x4*)(ta)), t1 = __builtin_amdgcn_ds_read_tr16_b64_v4i16((LAS s16x4*)(ta + 4*528));
        s16x4 t2 = __builtin_amdgcn_ds_read_tr16_b64_v4i16((LAS s16x4*)(ta + 8*528)), t3 = __builtin_amdgcn_ds_read_tr16_b64_v4i16((LAS s16x4*)(ta + 12*528));
        u32x2 d0 = __builtin_bit_cast(u32x2, t0), d1 = __builtin_bit_cast(u32x2, t1), d2 = __builtin_bit_cast(u32x2, t2), d3 = __builtin_bit_cast(u32x2, t3);
        const u32 rb = lds0 + L_X + kk*64; const u32 sw = (kk >> 3) & 3;
        *(LAS u32x4*)(rb + (2u ^ sw)*16) = u32x4{d0[0], d0[1], d1[0], d1[1]};
        *(LAS u32x4*)(rb + (3u ^ sw)*16) = u32x4{d2[0], d2[1], d3[0], d3[1]};
        if (wid == 4 && lane < 8) *(LAS u32*)(lds0 + L_X + 32 + lane*4) = pv;
        waitl<0>();
      } else if constexpr (K == 6) {
        u32x4 v[4];
        #pragma unroll
        for (int i = 0; i < 4; ++i) { const int pos = i*4096 + (tid - 256)*16, kk = pos >> 6, c = tid & 3;
          v[i] = *(const LAS u32x4*)(lds0 + L_X + kk*64 + ((c ^ ((kk >> 3) & 3))*16)); }
        char* dst = ws + WS_UW + (size_t)((tid - 256) >> 5)*131072 + b*512 + (((tid - 256)*16) & 511);
        #pragma unroll
        for (int i = 0; i < 4; ++i) st16_sc1(dst + (size_t)i*8*131072, v[i]);
      }
    }
  }
};
__device__ __forceinline__ void poll_lines(const char* p0, int nload, u32 e, int lane, char* ws){
  u32 sp = 0;
  for (; sp < SPIN_CAP; ++sp) {
    bool ok = true;
    for (int i = 0; i < nload; ++i) ok &= ld4_sc1(p0 + (size_t)(i*64 + lane)*128) == e;
    if (__all(ok)) break;
    __builtin_amdgcn_s_sleep(16);
  }
  if (sp == SPIN_CAP) TMO(5);
}

__device__ __forceinline__ void fft_stage(int kk, int wv_, int lane, u32 lds, char* ws){
  const char* src = ws + WS_UW + (size_t)(kk >> 3)*131072 + (size_t)wv_*64*512 + (kk & 7)*64;
  #pragma unroll
  for (int i = 0; i < 4; ++i) { const int ul = 16*i + (lane >> 2);
    dma16_sc1((u32)ul*512u + (u32)(((lane & 3) ^ ((ul >> 2) & 3))*16), src, lds + wv_*4096 + i*1024); }
}
struct ColTab { bf16x8 f[12]; };
__device__ __forceinline__ void col_tab_load(ColTab& t, int wv_, int lane){
  const char* p0 = DFT_PTR; const char* p1 = DFT_PTR + (size_t)(8 + 4*wv_)*1024; const u32 vo = lane*16;
  asm volatile("global_load_dwordx4 %0, %8, %9\n\tglobal_load_dwordx4 %1, %8, %9 offset:1024\n\tglobal_load_dwordx4 %2, %8, %9 offset:2048\n\tglobal_load_dwordx4 %3, %8, %9 offset:3072\n\t"
               "global_load_dwordx4 %4, %8, %10\n\tglobal_load_dwordx4 %5, %8, %10 offset:1024\n\tglobal_load_dwordx4 %6, %8, %10 offset:2048\n\tglobal_load_dwordx4 %7, %8, %10 offset:3072"
               : "=&v"(t.f[0]), "=&v"(t.f[1]), "=&v"(t.f[2]), "=&v"(t.f[3]), "=&v"(t.f[4]), "=&v"(t.f[5]), "=&v"(t.f[6]), "=&v"(t.f[7]) : "v"(vo), "s"(p0), "s"(p0 + 4096) : "memory");
  asm volatile("global_load_dwordx4 %0, %4, %5\n\tglobal_load_dwordx4 %1, %4, %5 offset:1024\n\tglobal_load_dwordx4 %2, %4, %5 offset:2048\n\tglobal_load_dwordx4 %3, %4, %5 offset:3072"
               : "=&v"(t.f[8]), "=&v"(t.f[9]), "=&v"(t.f[10]), "=&v"(t.f[11]) : "v"(vo), "s"(p1) : "memory");
}
__device__ __forceinline__ void col_tab_pin(ColTab& t){
  asm volatile("" : "+v"(t.f[0]), "+v"(t.f[1]), "+v"(t.f[2]), "+v"(t.f[3]), "+v"(t.f[4]), "+v"(t.f[5]), "+v"(t.f[6]), "+v"(t.f[7]), "+v"(t.f[8]), "+v"(t.f[9]), "+v"(t.f[10]), "+v"(t.f[11]));
}
__device__ __forceinline__ cf cm(cf a, cf b){ return cf{a.x*b.x - a.y*b.y, a.x*b.y + a.y*b.x}; }
__device__ __forceinline__ cf cis(float rev){ return cf{__builtin_amdgcn_cosf(rev), __builtin_amdgcn_sinf(rev)}; }
__device__ __forceinline__ bf16x8 frag_neg(bf16x8 a){ u32x4 v = __builtin_bit_cast(u32x4, a); v[0] ^= 0x80008000u; v[1] ^= 0x80008000u; v[2] ^= 0x80008000u; v[3] ^= 0x80008000u; return __builtin_bit_cast(bf16x8, v); }
#define MFMA16(a,b,c) __builtin_amdgcn_mfma_f32_16x16x32_bf16(a,b,c,0,0,0)

template<class SYNC>
__device__ __forceinline__ void fft_column(int kk, int u, u32 lds, u32 lm, char* ws, bool staged, u32 cdone, u32 hdone, ColTab& tb, bool tile, SYNC sync
){
  const int lane = u & 63, g = lane >> 4, r = lane & 15;
  const int w = __builtin_amdgcn_readfirstlane(u >> 6);
  if (!staged) fft_stage(kk, w, lane, lds, ws);
  u32x4 q0 = {}, q1 = {};
  if (kk == 0) { const __amdgpu_buffer_rsrc_t rQ = __builtin_amdgcn_make_buffer_rsrc(ws + WS_WQ, 0, 8192, 0x00020000);
    q0 = __builtin_amdgcn_raw_buffer_load_b128(rQ, u*32, 0, 16); q1 = __builtin_amdgcn_raw_buffer_load_b128(rQ, u*32 + 16, 0, 16); }
  cf twf[2][4], twi[2][4];
  #pragma unroll
  for (int c = 0; c < 2; ++c) {
    const int k1 = 16*c + r, n2 = 16*w + r;
    const cf sf = cis(-(float)k1*(1.0f/2048.0f)), si = cis((float)n2*(1.0f/2048.0f));
    twf[c][0] = cis(-(float)(k1*(16*w + 4*g))*(1.0f/2048.0f)); twi[c][0] = cis((float)(n2*(16*c + 4*g))*(1.0f/2048.0f));
    #pragma unroll
    for (int e = 1; e < 4; ++e) { twf[c][e] = cm(twf[c][e-1], sf); twi[c][e] = cm(twi[c][e-1], si); }
  }
  STAMP(1, 9);
  if (staged && tile && kk != 0) waitv<4>(); else waitv<0>();
  col_tab_pin(tb);
  sync();
  STAMP(1, 10);
  u32x4 w0, w1; bf16x8 xr, xi;
  { const int ur = 64*g + 16*w + r; const u32 rb = lds + ur*64, sw = (ur >> 2) & 3;
    xr = lds_row(rb + (0u ^ sw)*16); xi = lds_row(rb + (1u ^ sw)*16);
    const u32 rt = lds + u*64, st = (u >> 2) & 3;
    w0 = *(const LAS u32x4*)(rt + (2u ^ st)*16); w1 = *(const LAS u32x4*)(rt + (3u ^ st)*16); }
  waitv<0>(); lds_arrive(hdone);
  f32x4 aR[2], aI[2];
  { const bf16x8 nxr = frag_neg(xr);
    #pragma unroll
    for (int c = 0; c < 2; ++c) {
      aR[c] = MFMA16(xr, tb.f[2*c], (f32x4){}); aR[c] = MFMA16(xi, tb.f[2*c + 1], aR[c]);
      aI[c] = MFMA16(xi, tb.f[2*c], (f32x4){}); aI[c] = MFMA16(nxr, tb.f[2*c + 1], aI[c]);
    } }
  auto row = [&](int m, int plane, int k1) -> u32 { return m == 0 ? lm + plane*32768 + (6 + (k1 >> 4))*4096 + (k1 & 15)*128 : lds + plane*4096 + k1*128; };
  auto put = [&](int m){
    #pragma unroll
    for (int c = 0; c < 2; ++c) { const int k1 = 16*c + r; const u32 o = (((2*w + (g >> 1)) ^ (k1 & 7))*16) + 8*(g & 1);
      *(LAS u32x2*)(row(m, 0, k1) + o) = u32x2{pack2(aR[c][0], aR[c][1]), pack2(aR[c][2], aR[c][3])};
      *(LAS u32x2*)(row(m, 1, k1) + o) = u32x2{pack2(aI[c][0], aI[c][1]), pack2(aI[c][2], aI[c][3])}; }
  };
  auto get = [&](int m, int c, int ks, int plane){ const int k1 = 16*c + r; return lds_row(row(m, plane, k1) + (((4*ks + g) ^ (k1 & 7))*16)); };
  #pragma unroll
  for (int c = 0; c < 2; ++c)
    #pragma unroll
    for (int e = 0; e < 4; ++e) { const cf t = cm(cf{aR[c][e], aI[c][e]}, twf[c][e]); aR[c][e] = t.x; aI[c][e] = t.y; }
  put(0);
  sync();
  const bf16x8 nS0 = frag_neg(tb.f[9]), nS1 = frag_neg(tb.f[11]);
  #pragma unroll
  for (int c = 0; c < 2; ++c) {
    const bf16x8 tr0 = get(0, c, 0, 0), ti0 = get(0, c, 0, 1), tr1 = get(0, c, 1, 0), ti1 = get(0, c, 1, 1);
    aR[c] = MFMA16(tb.f[8], tr0, (f32x4){}); aR[c] = MFMA16(tb.f[9], ti0, aR[c]); aR[c] = MFMA16(tb.f[10], tr1, aR[c]); aR[c] = MFMA16(tb.f[11], ti1, aR[c]);
    aI[c] = MFMA16(tb.f[8], ti0, (f32x4){}); aI[c] = MFMA16(nS0, tr0, aI[c]);     aI[c] = MFMA16(tb.f[10], ti1, aI[c]); aI[c] = MFMA16(nS1, tr1, aI[c]);
  }
  STAMP(1, 11);
  const float sc = (kk == 0 ? 1.0f : 2.0f) * (1.0f/(2048.0f*512.0f));
  if (kk == 0) {
    cf zc[8];
    #pragma unroll
    for (int j = 0; j < 8; ++j) { const int f = 16*(j >> 2) + r + 32*(16*w + 4*g + (j & 3)); *(LAS cf*)(lds + f*8) = cf{aR[j >> 2][j & 3], aI[j >> 2][j & 3]}; }
    sync();
    #pragma unroll
    for (int j = 0; j < 8; ++j) { const int f = 16*(j >> 2) + r + 32*(16*w + 4*g + (j & 3)); zc[j] = *(const LAS cf*)(lds + ((S_ - f) & (S_ - 1))*8); }
    sync();
    #pragma unroll
    for (int j = 0; j < 8; ++j) {
      const u32 pw = j < 4 ? w0[j] : w1[j-4], qw = j < 4 ? q0[j] : q1[j-4];
      const cf t = cm(cf{aR[j >> 2][j & 3], aI[j >> 2][j & 3]}, cf{bflo(pw), bfhi(pw)}*sc) + cm(cf{zc[j].x, -zc[j].y}, cf{bflo(qw), bfhi(qw)}*sc);
      aR[j >> 2][j & 3] = t.x; aI[j >> 2][j & 3] = t.y;
    }
  } else {
    #pragma unroll
    for (int j = 0; j < 8; ++j) { const u32 pw = j < 4 ? w0[j] : w1[j-4];
      const cf t = cm(cf{aR[j >> 2][j & 3], aI[j >> 2][j & 3]}, cf{bflo(pw), bfhi(pw)}*sc); aR[j >> 2][j & 3] = t.x; aI[j >> 2][j & 3] = t.y; }
  }
  put(1);
  sync();
  lds_arrive(cdone);
  #pragma unroll
  for (int c = 0; c < 2; ++c) {
    const bf16x8 zr0 = get(1, c, 0, 0), zi0 = get(1, c, 0, 1), zr1 = get(1, c, 1, 0), zi1 = get(1, c, 1, 1);
    aR[c] = MFMA16(zr0, tb.f[8], (f32x4){}); aR[c] = MFMA16(zi0, nS0, aR[c]);     aR[c] = MFMA16(zr1, tb.f[10], aR[c]); aR[c] = MFMA16(zi1, nS1, aR[c]);
    aI[c] = MFMA16(zi0, tb.f[8], (f32x4){}); aI[c] = MFMA16(zr0, tb.f[9], aI[c]); aI[c] = MFMA16(zi1, tb.f[10], aI[c]); aI[c] = MFMA16(zr1, tb.f[11], aI[c]);
  }
  #pragma unroll
  for (int c = 0; c < 2; ++c)
    #pragma unroll
    for (int e = 0; e < 4; ++e) { const cf t = cm(cf{aR[c][e], aI[c][e]}, twi[c][e]); aR[c][e] = t.x; aI[c][e] = t.y; }
  const bf16x8 yr = __builtin_bit_cast(bf16x8, u32x4{pack2(aR[0][0], aR[0][1]), pack2(aR[0][2], aR[0][3]), pack2(aR[1][0], aR[1][1]), pack2(aR[1][2], aR[1][3])});
  const bf16x8 yi = __builtin_bit_cast(bf16x8, u32x4{pack2(aI[0][0], aI[0][1]), pack2(aI[0][2], aI[0][3]), pack2(aI[1][0], aI[1][1]), pack2(aI[1][2], aI[1][3])});
  #pragma unroll
  for (int c = 0; c < 2; ++c) {
    const bf16x8 nS = frag_neg(tb.f[5 + 2*c]);
    aR[c] = MFMA16(tb.f[4 + 2*c], yr, (f32x4){}); aR[c] = MFMA16(nS, yi, aR[c]);
    aI[c] = MFMA16(tb.f[4 + 2*c], yi, (f32x4){}); aI[c] = MFMA16(tb.f[5 + 2*c], yr, aI[c]);
  }
  STAMP(1, 12);
  const u32x4 ore = {pack2(aR[0][0], aR[0][1]), pack2(aR[0][2], aR[0][3]), pack2(aR[1][0], aR[1][1]), pack2(aR[1][2], aR[1][3])};
  const u32x4 oim = {pack2(aI[0][0], aI[0][1]), pack2(aI[0][2], aI[0][3]), pack2(aI[1][0], aI[1][1]), pack2(aI[1][2], aI[1][3])};
  const int up = 64*g + 16*w + r;
  const int cre = kk, cim = (kk == 0) ? 256 : 256 + kk;
  char* zb = ws + WS_AB + ((size_t)(up >> 3)*KT_ + F_)*128 + (up & 7)*16;
  st16_sc1(zb + (size_t)cre*128, ore);
  st16_sc1(zb + (size_t)cim*128, oim);
}

#define LA 6
#define WSLEEP 1
#define HSLEEP0 24
struct KGFrag { bf16x8 d0,d1,p0,p1; };
template<int I> struct StepLoop { template<class Fn> static __device__ __forceinline__ void run(Fn& f){ f.template step<I>(); StepLoop<I+1>::run(f); } };
template<> struct StepLoop<36> { template<class Fn> static __device__ __forceinline__ void run(Fn&){} };

static_assert(LA <= 6 && CDPOS <= 1, "ring slots 6, 7 hold the first exchange matrix of the column transform until cdone");
#define ZLA 3
#define NDR 8
constexpr int g2_younger(int I){
  const int j = I + 1; int n = 0; bool seen = false;
  if (j < LA) { seen = true; n = 2*(LA - 1 - j); }
  for (int st = 0; st < I; ++st) {
    const int a = st + LA, z = st + ZLA;
    if (a < 28) { if (seen) n += (a >= NDR + LA) ? 4 : 2; if (a == j) seen = true; }
    if (z >= 28 && z < 36) { if (seen) n += 2; if (z == j) seen = true; }
  }
  return n;
}
struct KG {
  const char* aT; const char* wd; u32 voP, voD, ring, rP0, rP1, zword; int rot;
  __device__ __forceinline__ int kidx(int i){ int k = i + rot; return k >= 28 ? k - 28 : k; }
  f32x16 acc00 = {}, acc01 = {}, acc10 = {}, acc11 = {};
  KGFrag F[2];
  bf16x8 dr[NDR][2];
  bf16x8 zd[8][2];
  __device__ __forceinline__ void issueD(int i){
    u32 l = ring + (i & 7)*4096 + 2048;
    const char* sd = wd + (size_t)kidx(i)*64*1024;
    dma16(voD, sd, l); dma16(voD, sd + 8*1024, l + 1024);
  }
  __device__ __forceinline__ void gen_zd(int w, int lane, int d0){
    #pragma unroll
    for (int t = 0; t < 2; ++t) {
      const int d = d0 + 32*t + (lane & 31);
      const float dq = (float)d*(1.0f/512.0f), k2 = 2.0f*__builtin_amdgcn_cosf(dq);
      #pragma unroll
      for (int i = 0; i < 8; ++i) {
        const int c0 = 16*(4*i + w) + 8*(lane >> 5);
        const int a = (c0 < 256 ? c0*d : (c0 - 256)*d + 128) & 511;
        const float r0 = (float)a*(1.0f/512.0f);
        float v[8];
        v[0] = __builtin_amdgcn_cosf(r0); v[1] = __builtin_amdgcn_cosf(r0 + dq);
        #pragma unroll
        for (int j = 2; j < 8; ++j) v[j] = fmaf(k2, v[j-1], -v[j-2]);
        if (i == 4) { if (c0 == 256) v[0] = (d & 1) ? -1.0f : 1.0f; }
        zd[i][t] = __builtin_bit_cast(bf16x8, u32x4{pack2(v[0], v[1]), pack2(v[2], v[3]), pack2(v[4], v[5]), pack2(v[6], v[7])});
      }
    }
    asm volatile("" : "+v"(zd[0][0]), "+v"(zd[0][1]), "+v"(zd[1][0]), "+v"(zd[1][1]), "+v"(zd[2][0]), "+v"(zd[2][1]), "+v"(zd[3][0]), "+v"(zd[3][1]),
                      "+v"(zd[4][0]), "+v"(zd[4][1]), "+v"(zd[5][0]), "+v"(zd[5][1]), "+v"(zd[6][0]), "+v"(zd[6][1]), "+v"(zd[7][0]), "+v"(zd[7][1]));
  }
  __device__ __forceinline__ void issueP(int i){
    u32 l = ring + (i & 7)*4096;
    const char* sp = aT + (size_t)(i < 28 ? kidx(i) : i)*64*128;
    dma16_sc1(voP, sp, l); dma16_sc1(voP, sp + 1024, l + 1024);
  }
  __device__ __forceinline__ KGFrag rd(int i){
    KGFrag f; u32 o = (i & 7)*4096;
    f.p0 = lds_tr(rP0 + o, rP0 + o + 512); f.p1 = lds_tr(rP1 + o, rP1 + o + 512);
    if (i < NDR) { f.d0 = dr[i < NDR ? i : 0][0]; f.d1 = dr[i < NDR ? i : 0][1]; }
    else if (i < 28) { f.d0 = lds_tr(rP0 + o + 2048, rP0 + o + 2048 + 512); f.d1 = lds_tr(rP1 + o + 2048, rP1 + o + 2048 + 512); }
    else { f.d0 = zd[i - 28][0]; f.d1 = zd[i - 28][1]; }
    return f;
  }
  __device__ __forceinline__ void load_dr(){
    #pragma unroll
    for (int i = 0; i < NDR; ++i) { const u32 o = (i & 7)*4096 + 2048;
      dr[i][0] = lds_tr(rP0 + o, rP0 + o + 512); dr[i][1] = lds_tr(rP1 + o, rP1 + o + 512); }
    #pragma unroll
    for (int i = 0; i < NDR; ++i) asm volatile("" : "+v"(dr[i][0]), "+v"(dr[i][1]) :: "memory");
  }
  __device__ __forceinline__ void wait_z(){
    u32 sp = 0;
    while (__builtin_amdgcn_readfirstlane(*(volatile LAS u32*)zword) == 0 && ++sp < SPIN_CAP) __builtin_amdgcn_s_sleep(1);
  }
  template<int I> __device__ __forceinline__ void step(){
    if constexpr (I + 1 < 36) {
      constexpr int N = g2_younger(I);
      waitv<N>();
      F[(I+1)&1] = rd(I + 1);
      if constexpr (I + 1 >= NDR && I + 1 < 28) waitl<8>(); else waitl<4>();
    } else waitl<0>();
    if constexpr (I == 0) PST(10); if constexpr (I == 2) PST(11); if constexpr (I == 4) PST(12); if constexpr (I == 8) PST(13); if constexpr (I == 16) PST(14); if constexpr (I == 28) PST(15); if constexpr (I == 35) PST(16);
    if constexpr (I == 8) STAMP(0, 16);

    if constexpr (I == 28) STAMP(0, 19);
    if constexpr (I + LA < 28) { issueP(I + LA); if constexpr (I + LA >= NDR + LA) issueD(I + LA); }
    if constexpr (I + ZLA == 28) { STAMP(0, 17); wait_z(); STAMP(0, 18); }
    if constexpr (I + ZLA >= 28 && I + ZLA < 36) issueP(I + ZLA);
    SCHEDB();
    const KGFrag& f = F[I&1];
    acc00 = MFMA(f.d0, f.p0, acc00); acc01 = MFMA(f.d0, f.p1, acc01); acc10 = MFMA(f.d1, f.p0, acc10); acc11 = MFMA(f.d1, f.p1, acc11);
    asm volatile("" : "+v"(acc00), "+v"(acc01), "+v"(acc10), "+v"(acc11));
    SCHEDB();
  }
};

__device__ __forceinline__ void dma4(u32 voff, const void* sbase, u32 lds){
  asm volatile("s_mov_b32 m0, %2\n\ts_nop 0\n\tglobal_load_lds_dword %0, %1" :: "v"(voff), "s"(sbase), "s"(lds) : "memory");
}
__device__ __forceinline__ void dma4_sc1(u32 voff, const void* sbase, u32 lds){
  asm volatile("s_mov_b32 m0, %2\n\ts_nop 0\n\tglobal_load_lds_dword %0, %1 sc1" :: "v"(voff), "s"(sbase), "s"(lds) : "memory");
}
__device__ __forceinline__ void add1_agent(void* p){ __hip_atomic_fetch_add((GAS u32*)p, 1u, __ATOMIC_RELAXED, __HIP_MEMORY_SCOPE_AGENT); }

__global__ __launch_bounds__(512,2) void fused(const u16* __restrict__ x, const u16* __restrict__ wr, const u16* __restrict__ wi,
                                               const u16* __restrict__ wg, const u16* __restrict__ wu, const u16* __restrict__ wd,
                                               char* __restrict__ ws, u16* __restrict__ out){
  extern __shared__ __attribute__((aligned(16))) char smem[];
  const u32 lds0 = (u32)(size_t)(LAS char*)smem;
  const int tid = threadIdx.x, lane = tid & 63;
  const int wid = __builtin_amdgcn_readfirstlane(tid >> 6);
  const int b = blockIdx.x, xcd = b & 7, idx = b >> 3;
  STAMP(0, 0); STAMP(1, 0); PST(0);
  if (tid < 16) *(LAS u32*)(lds0 + L_CNT + tid*4) = 0;
  lbar();
  if (wid < 4) { const char* ra = (const char*)x + (size_t)(b + 256*(2*wid))*1024; const u32 xb0 = lds0 + L_X + wid*4608;
    dma16(lane*16, ra, xb0); dma16(lane*16, ra + 256*1024, xb0 + 1024); }
  else if (wid == 6) {
    u64 pc; asm volatile("s_getpc_b64 %0" : "=s"(pc));
    pc &= ~(u64)255;
    if (idx*2048 < CODE_BYTES && lane < 32) {
      #pragma unroll
      for (int i = 0; i < 4; ++i) dma16(lane*16, (const char*)pc + idx*2048 + i*512, lds0 + L_DUM2); }
  }
  char* const fl = ws + WS_FL;
  const int pt = xcd*4 + (idx & 3), dt = idx >> 2;
  if (wid == 0) { const u32 fo = lane < 8 ? FL_Z + (xcd*8 + lane)*128 : (lane < 16 ? FL_U + (xcd*8 + lane - 8)*128 : (lane == 16 ? FL_H + (pt >> 1)*128 : FL_E));
    dma4_sc1(fo, fl, lds0 + L_BASE); }
  const bool tile = idx < 28;
  const int kcol = 32*xcd + ((idx + 4) & 31);
  bool staged = false;
  ColTab ctab;
  const int mt = 4*(xcd & 3) + (idx & 3), nt = 7*(xcd >> 2) + (idx >> 2);
  const int wm = wid >> 2, wn = wid & 3;

  Side sd; sd.lane = lane; sd.wid = wid; sd.tid = tid; sd.b = b; sd.lds0 = lds0; sd.ws = ws; sd.pv = 0;
  const int f0 = (b & 15) + 128*((b >> 4) & 3) + 512*(b >> 6);
  const u32 rs = F_*2;
  const char* gB = (const char*)wg + nt*256 + (size_t)(4*wid)*rs;
  const char* uB = (const char*)wu + nt*256 + (size_t)(4*wid)*rs;
  const char* xB = (const char*)x + (size_t)(mt*16 + 2*wid)*1024;
  const u32 voA0 = (u32)(lane >> 3)*262144u + (u32)(((lane & 7) ^ (lane >> 4))*16);
  const u32 voA1 = voA0 ^ 64u;
  const u32 voB = (u32)(lane >> 4)*rs + (u32)(((lane & 15) ^ ((lane >> 4) << 2))*16);
  const int rotA = 0;
  auto dmaA = [&](int j, int i){ dma16(i ? voA1 : voA0, xB + ((j + rotA) & 7)*128 + i*1024, lds0 + L_A + (j%3)*16384 + wid*2048 + i*1024); };
  auto dmaB = [&](int s, int i){ dma16(voB, (i ? uB : gB) + (size_t)(((s + 2*rotA) & 15)*32)*rs, lds0 + L_B + (s%NBS)*16384 + i*8192 + wid*1024); };
  auto fill_a = [&](){ dmaA(0,0); dmaA(0,1); dmaB(0,0); dmaB(0,1); dmaB(1,0); dmaB(1,1); };
  auto fill_b = [&](){ dmaA(1,0); dmaA(1,1); dmaB(2,0); dmaB(2,1); dmaB(3,0); dmaB(3,1); dmaA(2,0); dmaA(2,1); dmaB(4,0); dmaB(4,1); };
  u32 e, hbase;
  if (wid < 4) {
    const u32 xb0 = lds0 + L_X + wid*4608;
    PST(1);
    if (tile) fill_a();
    PST(2);
    sd.pre(); PST(3);
    STAMP(0, 1);
    if (tile) waitv<6>(); else waitv<0>();
    PST(4);
    STAMP(0, 2);
    if (wid == 0) {
      const u32 e0 = __builtin_amdgcn_readfirstlane(*(const LAS u32*)(lds0 + L_BASE + 17*4)) + 1u;
      if (lane == 0) st4_sc1(fl + FL_ST + b*128, e0);
    }
    const u32x4 g0 = *(const LAS u32x4*)(xb0 + lane*16), g1 = *(const LAS u32x4*)(xb0 + 1024 + lane*16);
    sd.g0 = g0; sd.g1 = g1;
    asm volatile("" : "+v"(sd.g0), "+v"(sd.g1) :: "memory");
    sd.run<0>(); PST(5); sd.run<1>(); PST(6); sd.run<2>(); sd.run<3>(); PST(7);
    if (tile) fill_b();
    { u32 sp = 0; while (__builtin_amdgcn_readfirstlane(*(volatile LAS u32*)(lds0 + L_CNT + 28)) < 4u && ++sp < SPIN_CAP) __builtin_amdgcn_s_sleep(1); }
    { const cf m2 = *(const LAS cf*)(lds0 + L_ST8 + wid*8), r2 = *(const LAS cf*)(lds0 + L_ST8 + 64 + wid*8); sd.st = RowStat{m2.x, r2.x, m2.y, r2.y}; }
    sd.run<4>();
    if (tile) waitv<10>();
    PST(8);
  } else {
    u32x4 c4[4], w0, w1, xa, xb; u32 sp;
    {
      const int j0 = 2*(wid - 4);
      const char* ra = (const char*)x + (size_t)(b + 256*j0)*1024;
      const char* a0 = ((lane < 32) ? (const char*)wr : (const char*)wi) + (size_t)(f0 + 32*(j0 & 3) + 16*(j0 >> 2))*(DF_*2) + (lane & 31)*16;
      const char* a1 = a0 + 32*(DF_*2);
      const int j = lane >> 3, c = lane & 7; const int f = f0 + 32*(j & 3) + 16*(j >> 2), fr = (c & 2) ? ((S_ - f) & (S_ - 1)) : f;
      const char* a2 = ((c & 1) ? (const char*)wi : (const char*)wr) + (size_t)fr*(DF_*2) + ((c & 4) ? 512 : 0);
      const int cg = mt & 3, kq = mt >> 2;
      const char* base2 = (const char*)((cg & 2) ? wu : wg) + (size_t)(128*kq + 32*(wid - 4))*(F_*2) + (tile ? nt*256 : 0) + (cg & 1)*128;
      const u32 vo2 = (u32)(lane >> 3)*(F_*2) + (lane & 7)*16;
      asm volatile("global_load_dwordx4 %0, %2, %3\n\tglobal_load_dwordx4 %1, %2, %4" : "=&v"(xa), "=&v"(xb) : "v"(lane*16), "s"(ra), "s"(ra + 256*1024) : "memory");
      if (tile) fill_a();
      asm volatile("global_load_dwordx4 %0, %4, %5\n\tglobal_load_dwordx4 %1, %4, %6\n\tglobal_load_dwordx4 %2, %4, %7\n\tglobal_load_dwordx4 %3, %4, %8"
                   : "=&v"(c4[0]), "=&v"(c4[1]), "=&v"(c4[2]), "=&v"(c4[3])
                   : "v"(vo2), "s"(base2), "s"(base2 + (size_t)8*(F_*2)), "s"(base2 + (size_t)16*(F_*2)), "s"(base2 + (size_t)24*(F_*2)) : "memory");
      PST(1);
      asm volatile("global_load_dwordx4 %0, %3, off\n\tglobal_load_dwordx4 %1, %4, off\n\tglobal_load_ushort %2, %5, off"
                   : "=&v"(w0), "=&v"(w1), "=&v"(sp) : "v"(a0), "v"(a1), "v"(a2) : "memory"); }
    PST(2); PST(3);
    STAMP(1, 1);
    if (tile) waitv<13>(); else waitv<7>();
    asm volatile("" : "+v"(xa), "+v"(xb));
    PST(4);
    STAMP(1, 2);
    { float ma, mb; r1_mean(xa, xb, ma, mb);
      const RowStat rst = r1_var(xa, xb, ma, mb);
      if (lane == 0) { const u32 sb = lds0 + L_ST8 + (wid - 4)*8;
        *(LAS cf*)(sb) = cf{rst.ma, rst.mb}; *(LAS cf*)(sb + 32) = cf{rst.ra*rst.ra, rst.rb*rst.rb}; *(LAS cf*)(sb + 64) = cf{rst.ra, rst.rb}; }
      waitl<0>(); lds_arrive(lds0 + L_CNT + 28); }
    waitv<3>();
    asm volatile("" : "+v"(c4[0]), "+v"(c4[1]), "+v"(c4[2]), "+v"(c4[3]));
    { float cs[8];
      #pragma unroll
      for (int j = 0; j < 4; ++j) {
        cs[2*j]   = (bflo(c4[0][j]) + bflo(c4[1][j])) + (bflo(c4[2][j]) + bflo(c4[3][j]));
        cs[2*j+1] = (bfhi(c4[0][j]) + bfhi(c4[1][j])) + (bfhi(c4[2][j]) + bfhi(c4[3][j]));
      }
      #pragma unroll
      for (int j = 0; j < 8; ++j) cs[j] += dppf<0x128>(cs[j]);
      const u32 pb = lds0 + L_CP + ((wid - 4)*4 + (lane >> 4))*256 + (lane & 7)*32;
      if ((lane & 8) == 0) {
        *(LAS f32x4*)(pb) = f32x4{cs[0], cs[1], cs[2], cs[3]};
        *(LAS f32x4*)(pb + 16) = f32x4{cs[4], cs[5], cs[6], cs[7]};
      } }
    PST(5); PST(6);
    if (tile) { fill_b(); waitv<10>(); } else waitv<0>();
    asm volatile("" : "+v"(w0), "+v"(w1), "+v"(sp));
    sd.g0 = w0; sd.g1 = w1; sd.sp16 = sp;
    sd.run<7>(); PST(7);
    if (!tile) {
      const int gw = (xcd*4 + idx - 28)*4 + wid - 4;
      #pragma unroll
      for (int i = 0; i < 14; ++i) { const int c2 = gw*14 + i; dma16(lane*16, (const char*)wd + (size_t)c2*1024, lds0 + wid*1024); }
    }
    PST(8);
  }
  lbar();
  sd.run<5>();
  lbar();
  e = __builtin_amdgcn_readfirstlane(*(const LAS u32*)(lds0 + L_BASE + 17*4)) + 1u;
  hbase = __builtin_amdgcn_readfirstlane(*(const LAS u32*)(lds0 + L_BASE + 16*4));
  auto publish_stats = [&](){
    if (wid == 1) {
      if (lane < 16) st4_sc1(ws + ((lane < 8) ? WS_STM : WS_STR) + (size_t)(8*b + (lane & 7))*4, *(const LAS u32*)(lds0 + L_ST8 + lane*4));
    } else if (wid == 5 && tile) {
      const u32 pb = lds0 + L_CP + lane*4;
      float v[16];
      #pragma unroll
      for (int i = 0; i < 16; ++i) v[i] = *(const LAS float*)(pb + i*256);
      const float sacc = (((v[0] + v[1]) + (v[2] + v[3])) + ((v[4] + v[5]) + (v[6] + v[7]))) + (((v[8] + v[9]) + (v[10] + v[11])) + ((v[12] + v[13]) + (v[14] + v[15])));
      const int cg = mt & 3, kq = mt >> 2;
      st4_sc1(ws + WS_CS + ((size_t)(nt*4 + kq)*256 + cg*64 + lane)*4, __float_as_uint(sacc));
    }
  };
  if (!tile) publish_stats();
  STAMP(0, 3); STAMP(1, 3); PST(9);
  if (!tile) {
    if (wid < 4) {
      #pragma unroll
      for (int i = 0; i < 6; ++i) dma16(lane*16, DFT_PTR + (wid*6 + i)*1024, lds0 + wid*1024); }
    sd.run<6>();
    waitv<0>(); bar();
    if (tid == 0) st4_sc1(fl + FL_S + b*128, e);
    if (wid == 4) { poll_lines(fl + FL_ST, 4, e, lane, ws);
      if (lane < 8) add1_agent(fl + FL_U + (lane*8 + xcd)*128); }
  }

  if (tile) {
    const int h = lane >> 5, r = lane & 31;
    const int sig = 16*(r >> 4) + 8*((r >> 2) & 1) + 4*((r >> 3) & 1) + (r & 3);
    u32 aoff[4];
    #pragma unroll
    for (int k = 0; k < 4; ++k) aoff[k] = lds0 + L_A + (u32)(wm*64 + sig)*128 + (u32)(((2*k + h) ^ ((sig >> 1) & 7))*16);
    const int q = (lane & 15) >> 2, pp = lane & 3, blk = (lane >> 4) & 1;
    const u32 boff = lds0 + L_B + (u32)(8*h + q)*256 + (u32)(((4*wn + 2*blk + (pp >> 1)) ^ (q << 2))*16) + 8*(pp & 1);
    auto rdA = [&](int s, int ks, int m){ int j = s >> 1, kk = (s & 1)*2 + ks; return lds_row(aoff[kk] + (j%3)*16384 + m*4096); };
    auto rdB = [&](int s, int ks, int t){ u32 bb = boff + (s%NBS)*16384 + t*8192 + ks*4096; return lds_tr(bb, bb + 1024); };
    f32x16 acc[2][2] = {};
    bf16x8 FA[2][2], FB[2][2];
    STAMP(0, 4);
    FA[0][0] = rdA(0,0,0); FA[0][1] = rdA(0,0,1); FB[0][0] = rdB(0,0,0); FB[0][1] = rdB(0,0,1);
#define MM(f,m,t) acc[m][t] = MFMA(FA[f][m], FB[f][t], acc[m][t])
    auto step = [&](auto SC) __attribute__((always_inline)) {
      constexpr int s = decltype(SC)::value;
      if constexpr (s > 0 && s < 15) { if constexpr (s <= 12) waitv<6>(); else if constexpr (s == 13) waitv<2>(); else waitv<0>(); bar(); }
      if constexpr (s == 2) { if (wid < 4) dma4_sc1(lane*128, fl + FL_ST + wid*64*128, lds0 + L_FS + wid*256); }
      if constexpr (s == 3) { if (wid == 0) { if (lane == 0) st4_sc1(fl + FL_S + b*128, e); } }
      if constexpr (s == 4) { if (wid == 5) {
        const u32x4 v = *(const LAS u32x4*)(lds0 + L_FS + lane*16);
        if (!__all(v[0] == e && v[1] == e && v[2] == e && v[3] == e)) poll_lines(fl + FL_ST, 4, e, lane, ws);
        if (lane < 8) add1_agent(fl + FL_U + (lane*8 + xcd)*128); } }
      const u32 sfo = (lane & 16) ? (u32)((((lane & 3) + 4*(nt % 7))*8 + ((lane >> 2) & 3) + 4*(nt / 7))*128) : (u32)((16*mt + (lane & 15))*128);
      if constexpr (s == 9) { if (wid == 0) dma4_sc1(sfo, fl + FL_S, lds0 + L_FS); }
      if constexpr (s == 12) {
        const u32 v = *(const LAS u32*)(lds0 + L_FS + lane*4);
        if (!__all(v == e)) { u32 sp = 0; for (; sp < SPIN_CAP; ++sp) { if (__all(ld4_sc1(fl + FL_S + sfo) == e)) break; __builtin_amdgcn_s_sleep(6); } if (sp == SPIN_CAP) { TMO(4);
 } }
        if (wid < 4) dma16_sc1(lane*16, ws + WS_CS + (size_t)(nt*4 + wid)*1024, lds0 + L_CS + wid*1024);
        else if (wid == 4) dma16_sc1((lane & 31)*16 + (lane >> 5)*8192, ws + WS_STM + (size_t)mt*512, lds0 + L_STM);
        else if (wid == 5) dma4_sc1((xcd*8 + (lane & 7))*128, fl + FL_U, lds0 + L_FS + 256);
      }
      if constexpr (s == 14) {
        if (wid >= 4) {
          const u32 v = *(const LAS u32*)(lds0 + L_FS + 256 + lane*4), ub = *(const LAS u32*)(lds0 + L_BASE + (8 + (lane & 7))*4);
          staged = __all(v - ub >= 32u);
          if (staged) fft_stage(kcol, wid - 4, lane, lds0 + L_X, ws);
          col_tab_load(ctab, wid - 4, lane);
        }
      }
      if constexpr (s == 0) sd.run<6>();
      if constexpr (s == 1) STAMP(0, 20); if constexpr (s == 2) STAMP(0, 21); if constexpr (s == 3) STAMP(0, 22); if constexpr (s == 5) STAMP(0, 23);
      if constexpr (s == 8) STAMP(0, 24); if constexpr (s == 12) STAMP(0, 25); if constexpr (s == 13) STAMP(0, 26); if constexpr (s == 14) STAMP(0, 27);
      constexpr bool dB = s >= 1 && s + NBS - 1 <= 15, dA = (s & 1) == 0 && s >= 2 && s <= 10;
      SCHEDB();
      MM(0,0,0); SCHEDB(); FA[1][0] = rdA(s,1,0); SCHEDB();
      MM(0,0,1); SCHEDB(); FB[1][0] = rdB(s,1,0); SCHEDB();
      MM(0,1,0); SCHEDB(); FB[1][1] = rdB(s,1,1); SCHEDB();
      MM(0,1,1); SCHEDB(); FA[1][1] = rdA(s,1,1); SCHEDB();
      MM(1,0,0); SCHEDB(); if (s < 15) FA[0][0] = rdA(s+1,0,0); if (dA) dmaA(s/2+2, 0); SCHEDB();
      MM(1,0,1); SCHEDB(); if (s < 15) FB[0][0] = rdB(s+1,0,0); if (dA) dmaA(s/2+2, 1); SCHEDB();
      MM(1,1,0); SCHEDB(); if (s < 15) FB[0][1] = rdB(s+1,0,1); if (dB) dmaB(s+NBS-1, 0); SCHEDB();
      MM(1,1,1); SCHEDB(); if (s < 15) FA[0][1] = rdA(s+1,0,1); if (dB) dmaB(s+NBS-1, 1); SCHEDB();
      if constexpr (s == 0) publish_stats();
    };
    static_for<16>(step);
    STAMP(0, 5);
    lbar();
    const float csg = (*(const LAS float*)(lds0 + L_CS + (wn*32 + r)*4) + *(const LAS float*)(lds0 + L_CS + 1024 + (wn*32 + r)*4))
                    + (*(const LAS float*)(lds0 + L_CS + 2048 + (wn*32 + r)*4) + *(const LAS float*)(lds0 + L_CS + 3072 + (wn*32 + r)*4));
    const float csu = (*(const LAS float*)(lds0 + L_CS + (128 + wn*32 + r)*4) + *(const LAS float*)(lds0 + L_CS + 1024 + (128 + wn*32 + r)*4))
                    + (*(const LAS float*)(lds0 + L_CS + 2048 + (128 + wn*32 + r)*4) + *(const LAS float*)(lds0 + L_CS + 3072 + (128 + wn*32 + r)*4));
    const u32 tb = lds0 + (wid >> 1)*32768 + (wid & 1)*8192;
    const cf cg2 = {csg, csg}, cu2 = {csu, csu};
    #pragma unroll
    for (int m = 0; m < 2; ++m)
      #pragma unroll
      for (int gp = 0; gp < 2; ++gp) {
        const u32 ro = (wm*64 + m*32 + gp*16 + h*8)*4;
        f32x4 mA = *(const LAS f32x4*)(lds0 + L_STM + ro), mB = *(const LAS f32x4*)(lds0 + L_STM + ro + 16);
        f32x4 rA = *(const LAS f32x4*)(lds0 + L_STR + ro), rB = *(const LAS f32x4*)(lds0 + L_STR + ro + 16);
        u32x4 o;
        #pragma unroll
        for (int e2 = 0; e2 < 4; ++e2) {
          const int i0 = 8*gp + 2*e2;
          const cf mm = e2 < 2 ? cf{mA[2*e2], mA[2*e2+1]} : cf{mB[2*e2-4], mB[2*e2-3]};
          const cf rr = e2 < 2 ? cf{rA[2*e2], rA[2*e2+1]} : cf{rB[2*e2-4], rB[2*e2-3]};
          const cf g2 = cf{acc[m][0][i0], acc[m][0][i0+1]} - mm*cg2, u2 = cf{acc[m][1][i0], acc[m][1][i0+1]} - mm*cu2;
          const cf h2 = (g2*u2)*rr;
          o[e2] = pack2(h2.x, h2.y);
        }
        *(LAS u32x4*)(tb + (r >> 4)*4096 + (r & 15)*128 + (((4*m + 2*gp + h) ^ (r & 7))*16)) = o;
      }
    char* hb = ws + WS_AB + ((size_t)(mt*2 + wm)*KT_ + nt*128 + wn*32)*128;
    #pragma unroll
    for (int i = 0; i < 4; ++i) {
      const int row = 8*i + (lane >> 3);
      u32x4 o = *(const LAS u32x4*)(tb + (i >> 1)*4096 + (i & 1)*1024 + lane*16);
      st16_sc1(hb + row*128 + (((lane & 7) ^ (row & 7))*16), o);
    }
    STAMP(0, 6);
  }

  STAMP(0, 7); STAMP(0, 12);
  STAMP(1, 4);
  const u32 zword = lds0 + L_CNT + 8, fcnt = lds0 + L_CNT, hdone = lds0 + L_CNT + 24;
  if (wid < 4) {
    KG g;
    const int w = wid;
    g.aT = ws + WS_AB + ((size_t)pt*KT_ + 16*w)*128;
    g.wd = (const char*)wd + (size_t)(16*w)*1024 + dt*128;
    const u32 ch = (u32)(((lane & 7) ^ (((lane >> 4) & 1) << 2))*16);
    g.voP = (u32)(lane >> 3)*128u + ch; g.voD = (u32)(lane >> 3)*1024u + ch;
    g.ring = lds0 + w*32768; g.zword = zword;
    g.rot = 0;
    const int h = lane >> 5, q = (lane & 15) >> 2, pp = lane & 3, blk = (lane >> 4) & 1;
    g.rP0 = g.ring + (u32)(8*h + q)*128 + (u32)(((2*blk + (pp >> 1)) ^ ((q & 2) << 1))*16) + 8*(pp & 1);
    g.rP1 = g.rP0 ^ 64u;
    waitv<0>(); lds_arrive(hdone);
    if (w == 0) {
      { u32 sp = 0; while (__builtin_amdgcn_readfirstlane(*(volatile LAS u32*)hdone) < 8u && ++sp < SPIN_CAP) __builtin_amdgcn_s_sleep(1); }
      if (tile && lane == 0) add1_agent(fl + FL_H + mt*128);
    }
    STAMP(0, 13);
    #pragma unroll
    for (int i = 0; i < NDR; ++i) g.issueD(i);
    STAMP(0, 14);
    g.gen_zd(w, lane, dt*64);
    waitv<0>();
    g.load_dr();
    #pragma unroll
    for (int i = NDR; i < NDR + LA; ++i) g.issueD(i);
    if (w == 0) {
      const char* hp = fl + FL_H + (pt >> 1)*128;
      u32 sp = 0; for (; sp < SPIN_CAP; ++sp) { u32 v = __builtin_amdgcn_readfirstlane(ld4_sc1(hp)); if (v - hbase >= 14u) break; __builtin_amdgcn_s_sleep(8); } if (sp == SPIN_CAP) TMO(1);
      if (lane == 0) *(volatile LAS u32*)(zword + 4) = 1u;
    } else {
      u32 sp = 0;
      while (__builtin_amdgcn_readfirstlane(*(volatile LAS u32*)(zword + 4)) == 0 && ++sp < SPIN_CAP) __builtin_amdgcn_s_sleep(WSLEEP);
    }
    asm volatile("" ::: "memory");
    STAMP(0, 15);
    #pragma unroll
    for (int i = 0; i < LA; ++i) g.issueP(i);
    waitv<2*(LA-1)>();
    g.F[0] = g.rd(0);
    STAMP(0, 8);
    { u32 sp = 0; while (__builtin_amdgcn_readfirstlane(*(volatile LAS u32*)(zword + 12)) < 4u && ++sp < SPIN_CAP) __builtin_amdgcn_s_sleep(WSLEEP); }
    STAMP(0, 9);
    StepLoop<0>::run(g);
    STAMP(0, 10);
    const int p = pt*64 + (w & 1)*32 + (lane & 31);
    const int s = (p >> 3) + 256*(p & 7);
    const int d0 = dt*64 + (w >> 1)*32;
    const size_t off0 = (size_t)s*D_ + d0 + 8*h;
    const u32x4 xv0 = *(const GAS u32x4*)(x + off0), xv1 = *(const GAS u32x4*)(x + off0 + 16);
    {
      auto put = [&](const f32x16& a, int slot){
        #pragma unroll
        for (int r = 0; r < 4; ++r) *(LAS f32x4*)(g.ring + slot*4096 + r*1024 + lane*16) = f32x4{a[4*r], a[4*r+1], a[4*r+2], a[4*r+3]};
      };
      if (w != 0) put(g.acc00, 0); if (w != 1) put(g.acc01, 1); if (w != 2) put(g.acc10, 2); if (w != 3) put(g.acc11, 3);
    }
    lbar();
    f32x16 a = w == 0 ? g.acc00 : w == 1 ? g.acc01 : w == 2 ? g.acc10 : g.acc11;
    #pragma unroll
    for (int o = 1; o < 4; ++o) {
      int ow = (w + o) & 3;
      #pragma unroll
      for (int r = 0; r < 4; ++r) { f32x4 v = *(const LAS f32x4*)(lds0 + ow*32768 + w*4096 + r*1024 + lane*16);
        a[4*r] += v[0]; a[4*r+1] += v[1]; a[4*r+2] += v[2]; a[4*r+3] += v[3]; }
    }
    #pragma unroll
    for (int gp = 0; gp < 2; ++gp) {
      float v[8];
      #pragma unroll
      for (int e2 = 0; e2 < 4; ++e2) {
        auto rr = __builtin_amdgcn_permlane32_swap(__float_as_uint(a[8*gp + e2]), __float_as_uint(a[8*gp + 4 + e2]), false, false);
        u32 lo = rr[0], hi = rr[1]; v[e2] = __uint_as_float(lo); v[4+e2] = __uint_as_float(hi);
      }
      const u32x4 xv = gp ? xv1 : xv0;
      u32x4 o;
      #pragma unroll
      for (int e2 = 0; e2 < 4; ++e2) o[e2] = pack2(bflo(xv[e2]) + v[2*e2], bfhi(xv[e2]) + v[2*e2+1]);
      *(GAS u32x4*)(out + off0 + 16*gp) = o;
    }
    if (b == 0 && tid == 0) st4_sc1(fl + FL_E, e);
    STAMP(0, 11);
    STAMP_FLUSH();
  } else {
    u32 gen = 0;
    auto sync = [&](){
      waitl<0>(); lds_arrive(fcnt); gen += 4;
      u32 sp = 0;
      while (__builtin_amdgcn_readfirstlane(*(volatile LAS u32*)fcnt) < gen && ++sp < SPIN_CAP) __builtin_amdgcn_s_sleep(1);
    };
    STAMP(1, 13);
    if (!tile) col_tab_load(ctab, wid - 4, lane);
    if (!staged) {
      if (wid == 4) {
        const char* up = fl + FL_U + (xcd*8 + (lane & 7))*128; const u32 ub = *(const LAS u32*)(lds0 + L_BASE + (8 + (lane & 7))*4);
        u32 sp = 0; for (; sp < SPIN_CAP; ++sp) { u32 v = ld4_sc1(up); if (__all(v - ub >= 32u)) break; __builtin_amdgcn_s_sleep(10); } if (sp == SPIN_CAP) TMO(2);
        if (lane == 0) *(volatile LAS u32*)(zword + 8) = 1u;
      } else {
        u32 sp = 0;
        while (__builtin_amdgcn_readfirstlane(*(volatile LAS u32*)(zword + 8)) == 0 && ++sp < SPIN_CAP) __builtin_amdgcn_s_sleep(2);
      }
      asm volatile("" ::: "memory");
    }
    STAMP(1, 5);
    __builtin_amdgcn_s_setprio(3);
    fft_column(kcol, tid - 256, lds0 + L_X, lds0, ws, staged, zword + 12, hdone, ctab, tile, sync);
    STAMP(1, 6);
    waitv<0>();
    sync();
    if (wid == 4) {
      if (lane < 8) add1_agent(fl + FL_Z + (lane*8 + xcd)*128);
      STAMP(1, 7);
      { const char* zp = fl + FL_Z + (xcd*8 + (lane & 7))*128;
        const u32 zb = *(const LAS u32*)(lds0 + L_BASE + (lane & 7)*4);
        __builtin_amdgcn_s_setprio(0);
#define ZSLEEP0 7
#define ZSLEEP 12
        for (int i = 0; i < ZSLEEP0; ++i) __builtin_amdgcn_s_sleep(10);
        u32 sp = 0; for (; sp < SPIN_CAP; ++sp) { u32 v = ld4_sc1(zp); if (__all(v - zb >= 32u)) break; __builtin_amdgcn_s_sleep(ZSLEEP); } if (sp == SPIN_CAP) TMO(3); }
      if (lane == 0) *(volatile LAS u32*)zword = 1u;
      STAMP(1, 8);
    }
    STAMP_FLUSH();
  }
}

extern "C" void kernel_launch(void* const* d_in, const int* in_sizes, int n_in,
                              void* d_out, int out_size, void* d_ws, size_t ws_size,
                              hipStream_t stream) {
  const u16* x=(const u16*)d_in[0]; const u16* wr=(const u16*)d_in[1]; const u16* wi=(const u16*)d_in[2];
  const u16* wg=(const u16*)d_in[4]; const u16* wu=(const u16*)d_in[5]; const u16* wd=(const u16*)d_in[6];
  (void)hipFuncSetAttribute((const void*)fused, hipFuncAttributeMaxDynamicSharedMemorySize, 163840);
  fused<<<256,512,163840,stream>>>(x,wr,wi,wg,wu,wd,(char*)d_ws,(u16*)d_out);
}
```
